```python
import torch

def _softmax(x, axis=-1):
    return torch.softmax(x, dim=axis)
def _gelu(x):
    return torch.nn.functional.gelu(x, approximate="tanh")
def _silu(x):
    return torch.nn.functional.silu(x)
def _sigmoid(x):
    return torch.sigmoid(x)
def _layernorm(x, axis=-1, eps=1e-5):
    m = x.mean(dim=axis, keepdim=True)
    v = x.var(dim=axis, keepdim=True, unbiased=False)
    return (x - m) / torch.sqrt(v + eps)
def _scatter_add(tgt, idx, src, axis=0):
    return tgt.clone().index_add_(axis, idx, src)
def _segment_sum(src, seg, n):
    out = torch.zeros((n,) + src.shape[1:], dtype=src.dtype)
    return out.index_add_(0, seg, src)
def _pad(x, pw):
    flat = tuple(v for pair in reversed(pw) for v in pair)
    return torch.nn.functional.pad(x, flat)

kernel_name = 'interleave_layers__sliding_block_cached__dense_activation_gelu__decode'

def setup_inputs():

    _w = (lambda a: (lambda t: t.to(torch.bfloat16) if t.is_floating_point() else t)(torch.as_tensor(a)))
    def _A_setup():
        B, Sq, D = 1, 1, 512
        Hq, Hkv, Sk, Dh = 32, 8, 65536, 128
        assert Hq % Hkv == 0, (Hq, Hkv)
        return {
            "x": _w((lambda *s: torch.randn(*s, dtype=torch.bfloat16) * 0.05)(B, Sq, D)),
            "k_cache": _w((lambda *s: torch.randn(*s, dtype=torch.bfloat16) * 0.8)(B, Hkv, Sk, Dh)),
            "v_cache": _w((lambda *s: torch.randn(*s, dtype=torch.bfloat16) * 1.6)(B, Hkv, Sk, Dh)),
            "wq": _w((lambda *s: torch.randn(*s, dtype=torch.bfloat16) * 0.8)(D, Hq * Dh) * (D ** -0.5)),
            "wo": _w((lambda *s: torch.randn(*s, dtype=torch.bfloat16) * 1.6)(Hq * Dh, D) * ((Hq * Dh) ** -0.5)),
        }
    _A_in = _A_setup()

    def _B_setup():
        B, S, D, F = 1, 1, 512, 1792
        return {
            "x": _w((lambda *s: torch.randn(*s, dtype=torch.bfloat16) * 0.05)(B, S, D)),
            "w1": _w((lambda *s: torch.randn(*s, dtype=torch.bfloat16) * 1.6)(D, F) * (D ** -0.5)),
            "b1": _w((lambda *s: torch.randn(*s, dtype=torch.bfloat16) * 1.6)(F) * 0.01),
            "w2": _w((lambda *s: torch.randn(*s, dtype=torch.bfloat16) * 0.2)(F, D) * (F ** -0.5)),
            "b2": _w((lambda *s: torch.randn(*s, dtype=torch.bfloat16) * 0.2)(D) * 0.01),
        }
    _B_in = _B_setup()

    return {
        "A_x": _A_in["x"],
        "A_k_cache": _A_in["k_cache"],
        "A_v_cache": _A_in["v_cache"],
        "A_wq": _A_in["wq"],
        "A_wo": _A_in["wo"],
        "B_x": _B_in["x"],
        "B_w1": _B_in["w1"],
        "B_b1": _B_in["b1"],
        "B_w2": _B_in["w2"],
        "B_b2": _B_in["b2"],
    }

def reference(A_x, A_k_cache, A_v_cache, A_wq, A_wo, B_x, B_w1, B_b1, B_w2, B_b2):
    def _A_ref(x, k_cache, v_cache, wq, wo):
        B, Sq, D = x.shape
        Hq, Dh = 32, 128
        Hkv, Sk = k_cache.shape[1], k_cache.shape[2]
        W = min(4096, max(256, Sk // 4))
        rep = Hq // Hkv
        q = (lambda x, *d: x.permute(*d))((x @ wq).reshape(B, Sq, Hq, Dh), 0, 2, 1, 3)
        k = (lambda x, n, axis: torch.repeat_interleave(x, n, dim=axis))(k_cache[:, :, Sk - W:, :], rep, axis=1)
        v = (lambda x, n, axis: torch.repeat_interleave(x, n, dim=axis))(v_cache[:, :, Sk - W:, :], rep, axis=1)
        scores = torch.einsum("bhqd,bhkd->bhqk", q, k) * (Dh ** -0.5)
        p = _softmax(scores, axis=-1)
        o = torch.einsum("bhqk,bhkd->bhqd", p, v)
        return (lambda x, *d: x.permute(*d))(o, 0, 2, 1, 3).reshape(B, Sq, Hq * Dh) @ wo

    def _B_ref(x, w1, b1, w2, b2):
        h = torch.einsum("bsd,df->bsf", x, w1) + b1
        h = _gelu(h)
        return torch.einsum("bsf,fd->bsd", h, w2) + b2

    x = A_x
    x = x + _A_ref(_layernorm(x, axis=-1), A_k_cache, A_v_cache, A_wq, A_wo)
    x = x + _B_ref(_layernorm(x, axis=-1), B_w1, B_b1, B_w2, B_b2)
    x = x + _A_ref(_layernorm(x, axis=-1), A_k_cache, A_v_cache, A_wq, A_wo)
    x = x + _B_ref(_layernorm(x, axis=-1), B_w1, B_b1, B_w2, B_b2)
    return x
```

```cpp
#include <hip/hip_runtime.h>
#include <hip/hip_bf16.h>
#include <stdint.h>

typedef unsigned short u16;
__device__ __forceinline__ float bf2f(u16 b) { return __uint_as_float(((unsigned)b) << 16); }
__device__ __forceinline__ u16 f2bf(float f) { unsigned u = __float_as_uint(f); return (u16)((u + 0x7fffu + ((u >> 16) & 1u)) >> 16); }
__device__ __forceinline__ float rbf(float f) { return bf2f(f2bf(f)); }

constexpr int D = 512, HQ = 32, HKV = 8, SK = 65536, DH = 128, W = 4096, F = 1792;
constexpr int NSPLIT = 32, KPS = W / NSPLIT;

__device__ void ln_block(const float* x, float* xn_s  , float* red  ) {
  int t = threadIdx.x;
  float a = x[t], b = x[t + 256];
  float s = a + b;
  for (int o = 32; o >= 1; o >>= 1) s += __shfl_xor(s, o);
  if ((t & 63) == 0) red[t >> 6] = s;
  __syncthreads();
  float m = (red[0] + red[1] + red[2] + red[3]) * (1.0f / D);
  __syncthreads();
  float da = a - m, db = b - m;
  float v = da * da + db * db;
  for (int o = 32; o >= 1; o >>= 1) v += __shfl_xor(v, o);
  if ((t & 63) == 0) red[t >> 6] = v;
  __syncthreads();
  float var = (red[0] + red[1] + red[2] + red[3]) * (1.0f / D);
  float mb = rbf(m), vb = rbf(var);
  float sd = rbf(sqrtf(rbf(vb + 1e-5f)));
  xn_s[t] = rbf(rbf(a - mb) / sd);
  xn_s[t + 256] = rbf(rbf(b - mb) / sd);
  __syncthreads();
}

__global__ void k_init(const u16* x, float* xf) { int t = threadIdx.x + blockIdx.x * 256; if (t < D) xf[t] = bf2f(x[t]); }

template <int MODE>
__global__ void k_ln_gemv(const float* x, const u16* Wt, int N, const u16* bias, float* y) {
  __shared__ float xn[D]; __shared__ float red[4]; __shared__ float part[4][64];
  ln_block(x, xn, red);
  int t = threadIdx.x, c = t & 63, g = t >> 6;
  int n = blockIdx.x * 64 + c;
  float acc = 0.f;
  for (int k = g * 128; k < g * 128 + 128; ++k) acc += xn[k] * bf2f(Wt[(size_t)k * N + n]);
  part[g][c] = acc;
  __syncthreads();
  if (g == 0) {
    float s = rbf(part[0][c] + part[1][c] + part[2][c] + part[3][c]);
    if (MODE == 1) {
      s = rbf(s + bf2f(bias[n]));
      float u = 0.7978845608028654f * (s + 0.044715f * s * s * s);
      s = rbf(0.5f * s * (1.0f + tanhf(u)));
    }
    y[n] = s;
  }
}

__global__ void k_attn(const float* q, const u16* kc, const u16* vc, float* pm, float* pl, float* po) {
  int s = blockIdx.x, g = blockIdx.y, t = threadIdx.x;
  __shared__ float qs[512]; __shared__ float sc[4][KPS]; __shared__ float mm[4], ll[4];
  qs[t] = q[g * 512 + t]; qs[t + 256] = q[g * 512 + t + 256];
  __syncthreads();
  const u16* kb = kc + ((size_t)g * SK + (SK - W) + s * KPS) * DH;
  const u16* vb = vc + ((size_t)g * SK + (SK - W) + s * KPS) * DH;
  for (int i = t; i < 4 * KPS; i += 256) {
    int h = i / KPS, key = i % KPS; float a = 0.f;
    for (int d = 0; d < DH; ++d) a += qs[h * 128 + d] * bf2f(kb[key * DH + d]);
    sc[h][key] = rbf(rbf(a) * 0.08838834764831845f);
  }
  __syncthreads();
  if (t < 4) { float m = -INFINITY; for (int k = 0; k < KPS; ++k) m = fmaxf(m, sc[t][k]); float l = 0.f; for (int k = 0; k < KPS; ++k) { float e = __expf(sc[t][k] - m); sc[t][k] = e; l += e; } mm[t] = m; ll[t] = l; }
  __syncthreads();
  for (int i = t; i < 512; i += 256) {
    int h = i / 128, d = i % 128; float a = 0.f;
    for (int k = 0; k < KPS; ++k) a += sc[h][k] * bf2f(vb[k * DH + d]);
    po[((size_t)(g * 4 + h) * NSPLIT + s) * 128 + d] = a;
  }
  if (t < 4) { pm[(g * 4 + t) * NSPLIT + s] = mm[t]; pl[(g * 4 + t) * NSPLIT + s] = ll[t]; }
}

__global__ void k_combine(const float* pm, const float* pl, const float* po, float* o) {
  int h = blockIdx.x, d = threadIdx.x;
  float M = -INFINITY; for (int s = 0; s < NSPLIT; ++s) M = fmaxf(M, pm[h * NSPLIT + s]);
  float L = 0.f, a = 0.f;
  for (int s = 0; s < NSPLIT; ++s) { float w = __expf(pm[h * NSPLIT + s] - M); L += w * pl[h * NSPLIT + s]; a += w * po[((size_t)h * NSPLIT + s) * 128 + d]; }
  o[h * 128 + d] = rbf(a / L);
}

__global__ void k_gemv_res(const float* v, int K, const u16* Wt, const u16* bias, const float* xin, float* xout, u16* outbf) {
  __shared__ float part[16][64];
  int t = threadIdx.x, c = t & 63, g = t >> 6; int n = blockIdx.x * 64 + c;
  int kp = K / 16; float acc = 0.f;
  for (int k = g * kp; k < (g + 1) * kp; ++k) acc += v[k] * bf2f(Wt[(size_t)k * D + n]);
  part[g][c] = acc; __syncthreads();
  if (g == 0) { float s = 0.f; for (int i = 0; i < 16; ++i) s += part[i][c]; s = rbf(s); if (bias) s = rbf(s + bf2f(bias[n])); float r = rbf(xin[n] + s); xout[n] = r; if (outbf) outbf[n] = f2bf(r); }
}

extern "C" void kernel_launch(void* const* d_in, const int* in_sizes, int n_in, void* d_out, int out_size, void* d_ws, size_t ws_size, hipStream_t stream) {
  const u16* x = (const u16*)d_in[0]; const u16* kc = (const u16*)d_in[1]; const u16* vc = (const u16*)d_in[2];
  const u16* wq = (const u16*)d_in[3]; const u16* wo = (const u16*)d_in[4];
  const u16* w1 = (const u16*)d_in[6]; const u16* b1 = (const u16*)d_in[7]; const u16* w2 = (const u16*)d_in[8]; const u16* b2 = (const u16*)d_in[9];
  float* ws = (float*)d_ws;
  float* xa = ws; float* xb = ws + 512; float* q = ws + 1024; float* o = q + 4096; float* h = o + 4096; float* pm = h + 2048; float* pl = pm + 1024; float* po = pl + 1024;
  k_init<<<2, 256, 0, stream>>>(x, xa);
  for (int l = 0; l < 2; ++l) {
    k_ln_gemv<0><<<64, 256, 0, stream>>>(xa, wq, 4096, nullptr, q);
    k_attn<<<dim3(NSPLIT, HKV), 256, 0, stream>>>(q, kc, vc, pm, pl, po);
    k_combine<<<32, 128, 0, stream>>>(pm, pl, po, o);
    k_gemv_res<<<8, 1024, 0, stream>>>(o, 4096, wo, nullptr, xa, xb, nullptr);
    k_ln_gemv<1><<<28, 256, 0, stream>>>(xb, w1, F, b1, h);
    k_gemv_res<<<8, 1024, 0, stream>>>(h, F, w2, b2, xb, xa, l == 1 ? (u16*)d_out : nullptr);
  }
}
```

```cpp
#include <hip/hip_runtime.h>
#include <stdint.h>

typedef unsigned u32;
typedef unsigned short u16;
typedef unsigned long long u64;
typedef u32 v4u __attribute__((ext_vector_type(4)));
typedef u32 v2u __attribute__((ext_vector_type(2)));
typedef float v4f __attribute__((ext_vector_type(4)));
typedef short v4s __attribute__((ext_vector_type(4)));
typedef __bf16 v8bf __attribute__((ext_vector_type(8)));
typedef __bf16 v2bf __attribute__((ext_vector_type(2)));
#define LAS __attribute__((address_space(3)))

#define STAMPS 0
#define D_E1 6
#define D_E2 6
#define D_E3 6
#define D_E4 6
#define D_E5 6
#define D_E6 6
#define DMA_POL " nt"

namespace {

constexpr int SK = 65536, WIN = 4096;
constexpr int NWG = 256, NTHR = 512;

constexpr u32 L_K = 0, L_V = 32768, L_WQ = 65536, L_WR = 81920, L_QS = 147456, L_XS = 148480, L_XP = 149504,
              L_P = 150528, L_ML = 151552, L_RED = 151808, L_B = 152320, L_CNT = 153344, L_ST = 153600, L_END = 153600 + 4096;

constexpr u32 X_E1 = 0;
constexpr u32 X_E2 = X_E1 + 512 * 128;
constexpr u32 X_E3 = X_E2 + 4352 * 128;
constexpr u32 X_E4 = X_E3 + 128 * 128;
constexpr u32 X_E5 = X_E4 + 64 * 128;
constexpr u32 X_E6 = X_E5 + 56 * 128;
constexpr u32 X_LAYER = X_E6 + 32 * 128;
constexpr u32 X_REGION = 2u << 20;
constexpr u32 X_NREG = 64;
constexpr u32 X_BASE = 2u << 20;
constexpr u32 X_DBG = X_BASE + X_NREG * X_REGION;
static_assert(2 * X_LAYER <= X_REGION, "region");

constexpr int SPIN_CAP = 1 << 18;

__device__ __forceinline__ void dma16(const void* gsrc, u32 lds_dst) {
  u32 keep;
  asm volatile("s_mov_b32 %0, m0\n\ts_mov_b32 m0, %2\n\ts_nop 0\n\tglobal_load_lds_dwordx4 %1, off" DMA_POL "\n\ts_mov_b32 m0, %0"
               : "=&s"(keep) : "v"(gsrc), "s"(lds_dst) : "memory");
}
__device__ __forceinline__ void lds_barrier() {
  asm volatile("s_waitcnt lgkmcnt(0)\n\ts_barrier" ::: "memory");
}
__device__ __forceinline__ float dot2(u32 a, u32 b, float c) {
  return __builtin_amdgcn_fdot2_f32_bf16(__builtin_bit_cast(v2bf, a), __builtin_bit_cast(v2bf, b), c, false);
}
__device__ __forceinline__ u32 pack_bf16(float lo, float hi) {
  u32 r;
  asm("v_cvt_pk_bf16_f32 %0, %1, %2" : "=v"(r) : "v"(lo), "v"(hi));
  return r;
}
__device__ __forceinline__ float bf_lo(u32 p) { return __uint_as_float(p << 16); }
__device__ __forceinline__ float bf_hi(u32 p) { return __uint_as_float(p & 0xffff0000u); }
__device__ __forceinline__ float rbf(float x) { return bf_lo(pack_bf16(x, x)); }
__device__ __forceinline__ u32 perm_lo(u32 a, u32 b) { return __builtin_amdgcn_perm(b, a, 0x05040100u); }
__device__ __forceinline__ u32 perm_hi(u32 a, u32 b) { return __builtin_amdgcn_perm(b, a, 0x07060302u); }
__device__ __forceinline__ float rdlane(float x, int l) { return __builtin_bit_cast(float, __builtin_amdgcn_readlane(__builtin_bit_cast(int, x), l)); }

template <int CTRL> __device__ __forceinline__ float dppf(float x) {
  return __builtin_bit_cast(float, __builtin_amdgcn_mov_dpp(__builtin_bit_cast(int, x), CTRL, 0xf, 0xf, true));
}
constexpr int XOR1 = 0xB1, XOR2 = 0x4E, XOR7 = 0x141, ROR8 = 0x128, ROR4 = 0x124, ROR2 = 0x122, ROR1 = 0x121;
constexpr u32 ONES = 0x3f803f80u;

__device__ __forceinline__ float tadd32(float a, float b) {
  auto s = __builtin_amdgcn_permlane32_swap(__float_as_uint(a), __float_as_uint(b), false, false);
  return __uint_as_float(s[0]) + __uint_as_float(s[1]);
}
__device__ __forceinline__ float tadd16(float a, float b) {
  auto s = __builtin_amdgcn_permlane16_swap(__float_as_uint(a), __float_as_uint(b), false, false);
  return __uint_as_float(s[0]) + __uint_as_float(s[1]);
}
__device__ __forceinline__ float tmax32(float a, float b) {
  auto s = __builtin_amdgcn_permlane32_swap(__float_as_uint(a), __float_as_uint(b), false, false);
  return fmaxf(__uint_as_float(s[0]), __uint_as_float(s[1]));
}
__device__ __forceinline__ float tmax16(float a, float b) {
  auto s = __builtin_amdgcn_permlane16_swap(__float_as_uint(a), __float_as_uint(b), false, false);
  return fmaxf(__uint_as_float(s[0]), __uint_as_float(s[1]));
}
__device__ __forceinline__ float rowsum16(float x) {
  x += dppf<ROR8>(x); x += dppf<ROR4>(x); x += dppf<ROR2>(x); x += dppf<ROR1>(x);
  return x;
}
__device__ __forceinline__ float reduce8(const float (&a)[8], int lane) {
  float t4[4], t2[2];
#pragma unroll
  for (int c = 0; c < 4; ++c) t4[c] = tadd32(a[c], a[c + 4]);
#pragma unroll
  for (int c = 0; c < 2; ++c) t2[c] = tadd16(t4[c], t4[c + 2]);
  const float lo = t2[0] + dppf<ROR8>(t2[0]), hi = t2[1] + dppf<ROR8>(t2[1]);
  float r = (lane & 8) ? hi : lo;
  r += dppf<XOR7>(r); r += dppf<XOR2>(r); r += dppf<XOR1>(r);
  return r;
}

template <int N>
__device__ __forceinline__ int gather(__amdgpu_buffer_rsrc_t rs, const u32 (&off)[N], u32 tag, v4u (&v)[N], bool& dead) {
  int spin = 0;
  for (;; ++spin) {
    asm volatile("" ::: "memory");
#pragma unroll
    for (int i = 0; i < N; ++i) v[i] = __builtin_amdgcn_raw_buffer_load_b128(rs, off[i], 0, 16);
    bool ok = true;
#pragma unroll
    for (int i = 0; i < N; ++i) ok &= (v[i].y == tag) & (v[i].w == tag);
    if (__all(ok)) break;
    if (dead || spin > SPIN_CAP) { dead = true; break; }
  }
  return spin;
}

struct Args {
  const u16* x; const u16* kc; const u16* vc; const u16* wq; const u16* wo;
  const u16* w1; const u16* b1; const u16* w2; const u16* b2;
  u16* out; unsigned char* ws;
};

#define STAMP(i) do {} while (0)
#define STAMPN(i, n) do { (void)(n); } while (0)

__global__ void __launch_bounds__(NTHR, 2) fused_decode(Args a) {
  __shared__ __attribute__((aligned(16))) unsigned char smem[L_END];
  LAS unsigned char* L = (LAS unsigned char*)smem;
  const u32 lbase = (u32)(size_t)L;
  const int tid = threadIdx.x, lane = tid & 63, wave = __builtin_amdgcn_readfirstlane(tid >> 6);
  const int b = blockIdx.x, g = b & 7, j = b >> 3;
  STAMP(0);

  const int qkh = j >> 4, qcg = j & 15;
  const bool isC = j < 16, isO = (j >= 16) & (j < 24), isH = (j >= 24) & (j < 31), isD = j < 4;
  const int cidx = j & 15;
  const int okh = (j >> 2) & 1;
  const int ocg = 4 * g + (j & 3);
  const int hidx = g * 7 + (isH ? j - 24 : 0);
  const int didx = 4 * g + (j & 3);

  u32 epoch;
  if (wave < 4) {
#pragma unroll
    for (int t = 0; t < 4; ++t) {
      const int lp = t * 16 + (lane >> 2), row = 256 * qkh + 4 * lp + wave;
      dma16((const char*)a.wq + (size_t)row * 8192 + g * 1024 + qcg * 64 + (lane & 3) * 16, lbase + L_WQ + (wave * 4 + t) * 1024);
    }
    if (wave == 0) dma16((const char*)a.x + lane * 16, lbase + L_XS);
    if (wave == 1) {
      const char* src = (lane & 7) < 4 ? (const char*)a.b1 + hidx * 64 + (lane & 3) * 16 : (const char*)a.b2 + didx * 32 + (lane & 1) * 16;
      dma16(src, lbase + L_B);
    }
    if (tid == 128) *(LAS u32*)(L + L_CNT) = 0u;
  }
  asm volatile("s_waitcnt lgkmcnt(0)\n\ts_load_dword %0, %1, 0x0\n\ts_barrier" : "=s"(epoch) : "s"(a.ws) : "memory");
  if (wave >= 4) {
    const size_t kvoff = ((size_t)g * SK + (SK - WIN) + j * 128) * 256;
#pragma unroll
    for (int t = 0; t < 8; ++t) {
      const int ins = t * 4 + (wave - 4), row = ins * 4 + (lane >> 4), ch = (lane & 15) ^ (row & 15);
      dma16((const char*)a.kc + kvoff + row * 256 + ch * 16, lbase + L_K + ins * 1024);
    }
#pragma unroll
    for (int t = 0; t < 8; ++t) {
      const int ins = t * 4 + (wave - 4), row = ins * 4 + (lane >> 4), ch = (lane & 15) ^ ((row & 7) << 1);
      dma16((const char*)a.vc + kvoff + row * 256 + ch * 16, lbase + L_V + ins * 1024);
    }
  }
  asm volatile("s_waitcnt lgkmcnt(0)" : "+s"(epoch) :: "memory");
  const u32 tag = epoch + 1u;
  const u32 xreg = X_BASE + (epoch % X_NREG) * X_REGION;
  const __amdgpu_buffer_rsrc_t rs = __builtin_amdgcn_make_buffer_rsrc(a.ws + xreg, 0, X_REGION, 0x00020000);

  auto load_role_weights = [&]() {
    if (isO | isD) {
      const char* wb = isO ? (const char*)a.wo + (size_t)okh * 2048 * 1024 + ocg * 32 : (const char*)a.w2 + didx * 32;
      const int nins = isO ? 64 : 56;
#pragma unroll
      for (int t = 0; t < 8; ++t) {
        if (t < 7 || isO) {
          int ins = (j & 3) * (nins >> 2) + t * 8 + wave;
          if (ins >= nins) ins -= nins;
          const int Pl = (ins & 7) * 32 + (lane >> 1), row = 256 * (ins >> 3) + 4 * (Pl & 63) + (Pl >> 6);
          dma16(wb + (size_t)row * 1024 + (lane & 1) * 16, lbase + L_WR + ins * 1024);
        }
      }
    } else if (isH) {
#pragma unroll
      for (int t = 0; t < 4; ++t) {
        const int tt = (t + 2 * (hidx & 1)) & 3;
        const int lp = tt * 16 + (lane >> 2), row = 8 * lp + wave;
        dma16((const char*)a.w1 + (size_t)row * 3584 + hidx * 64 + (lane & 3) * 16, lbase + L_WR + (wave * 4 + tt) * 1024);
      }
    }
  };

  bool dead = false;
  v4u kf[4]; v2u vf[8];
  STAMP(1);

  for (int layer = 0; layer < 2; ++layer) {
    const u32 xl = layer * X_LAYER;
    if (wave < 4) {
      if (layer == 0) {
        asm volatile("s_waitcnt vmcnt(0)" ::: "memory");
        if (lane == 0) __hip_atomic_fetch_add((LAS u32*)(L + L_CNT), 1u, __ATOMIC_RELAXED, __HIP_MEMORY_SCOPE_WORKGROUP);
        while (__hip_atomic_load((LAS u32*)(L + L_CNT), __ATOMIC_RELAXED, __HIP_MEMORY_SCOPE_WORKGROUP) < 4u) {}
        asm volatile("" ::: "memory");
      }
      u32 wr[2][8];
      {
        v4u r[4];
#pragma unroll
        for (int i = 0; i < 4; ++i) r[i] = *(const LAS v4u*)(L + L_WQ + (i * 64 + lane) * 64 + wave * 16);
#pragma unroll
        for (int p = 0; p < 2; ++p)
#pragma unroll
          for (int c = 0; c < 4; ++c) { wr[p][2 * c] = perm_lo(r[2 * p][c], r[2 * p + 1][c]); wr[p][2 * c + 1] = perm_hi(r[2 * p][c], r[2 * p + 1][c]); }
      }
      float cs;
      {
        float c[8];
#pragma unroll
        for (int q = 0; q < 8; ++q) c[q] = dot2(wr[1][q], ONES, dot2(wr[0][q], ONES, 0.f));
        cs = reduce8(c, lane);
      }
      u32 xv[4];
      if (layer == 0) {
        const v2u t0 = *(const LAS v2u*)(L + L_XS + lane * 8), t1 = *(const LAS v2u*)(L + L_XS + 512 + lane * 8);
        xv[0] = t0.x; xv[1] = t0.y; xv[2] = t1.x; xv[3] = t1.y;
        STAMP(2);
      } else {
        __builtin_amdgcn_s_sleep(D_E6);
        const u32 o0 = X_E6 + (lane >> 2) * 128 + (lane & 3) * 16;
        const u32 off[2] = {o0, o0 + 16 * 128};
        v4u v[2];
        const int n = gather<2>(rs, off, tag, v, dead);
        STAMPN(16, n);
        xv[0] = v[0].x; xv[1] = v[0].z; xv[2] = v[1].x; xv[3] = v[1].z;
        if (wave == 0) { *(LAS v2u*)(L + L_XS + lane * 8) = (v2u){xv[0], xv[1]}; *(LAS v2u*)(L + L_XS + 512 + lane * 8) = (v2u){xv[2], xv[3]}; }
      }
      const u32 xa = qkh ? xv[2] : xv[0], xb = qkh ? xv[3] : xv[1];
      float ac[8];
#pragma unroll
      for (int q = 0; q < 8; ++q) ac[q] = dot2(xb, wr[1][q], dot2(xa, wr[0][q], 0.f));
      float s1 = 0.f, s2 = 0.f;
#pragma unroll
      for (int p = 0; p < 4; ++p) { s1 = dot2(xv[p], ONES, s1); s2 = dot2(xv[p], xv[p], s2); }
      const float acc = reduce8(ac, lane);
      float ss = tadd32(s1, s2); ss = tadd16(ss, ss); ss = rowsum16(ss);
      const float mean = rdlane(ss, 0) * (1.f / 512.f);
      const float var = fmaxf(rdlane(ss, 32) * (1.f / 512.f) - mean * mean, 0.f);
      const float rstd = __builtin_amdgcn_rsqf(var + 1e-5f);
      const float y = (acc - mean * cs) * rstd;
      if ((lane & 7) == 0) {
        const v2u d = {__float_as_uint(y), tag};
        __builtin_amdgcn_raw_buffer_store_b64(d, rs, xl + X_E1 + (u32)(g * 32 + j) * 256 + wave * 64 + lane, 0, 16);
      }
      STAMP(3 + layer * 14);
      if (layer == 0) {
        const v4u z = {0u, ~tag, 0u, ~tag};
#pragma unroll
        for (int rep = 0; rep < 2; ++rep) {
          const int pc = tid + rep * 256;
          const int line = pc >> 3, lay = line >= 21, li = lay ? line - 21 : line;
          u32 o; bool act = pc < 336;
          if (li < 17) o = X_E2 + (u32)((g * 32 + j) * 17 + li) * 128;
          else if (li < 19) { o = X_E1 + (u32)((g * 32 + j) * 2 + li - 17) * 128; act &= (lay != 0); }
          else if (li == 19) { o = X_E3 + (u32)(g * 16 + cidx) * 128; act &= isC; }
          else { o = isO ? X_E4 + (u32)(okh * 32 + ocg) * 128 : isH ? X_E5 + (u32)hidx * 128 : X_E6 + (u32)didx * 128; act &= (isO | isH | isD); }
          if (act) __builtin_amdgcn_raw_buffer_store_b128(z, rs, o + lay * X_LAYER + (pc & 7) * 16, 0, 16);
        }
      }
    }
    if (layer != 0) lds_barrier();

    {
      const int gq = lane >> 4, hq = lane & 3, kr = lane & 15;
      if (layer == 0) {
        if (wave < 4) {
          __builtin_amdgcn_s_sleep(8);
          const u32 o0 = xl + X_E1 + (u32)(g * 64 + (lane >> 5) * 32 + wave * 8) * 128 + (lane & 31) * 16;
          const u32 off[2] = {o0, o0 + 512};
          v4u v[2];
          const int n = gather<2>(rs, off, tag, v, dead);
          STAMPN(4, n);
#pragma unroll
          for (int i = 0; i < 2; ++i) {
            float pA = __uint_as_float(v[i].x), pB = __uint_as_float(v[i].z);
            pA = tadd32(pA, pA); pB = tadd32(pB, pB);
            if (lane < 32) *(LAS u32*)(L + L_QS + (wave * 64 + i * 32 + lane) * 4) = pack_bf16(pA, pB);
          }
        } else {
          asm volatile("s_waitcnt vmcnt(0)" ::: "memory");
          STAMP(4);
        }
        lds_barrier();
        if (!isD) load_role_weights();
#pragma unroll
        for (int s = 0; s < 4; ++s) kf[s] = *(const LAS v4u*)(L + L_K + (wave * 16 + kr) * 256 + (((4 * s + gq) ^ kr) * 16));
        const int vq = (lane & 15) >> 2, vp = lane & 3;
        const int row0 = 4 * gq + vq;
        const u32 va = lbase + L_V + row0 * 256 + (((2 * wave + (vp >> 1)) ^ ((row0 & 7) << 1)) * 16) + (vp & 1) * 8;
        asm volatile(
            "ds_read_b64_tr_b16 %0, %8\n\tds_read_b64_tr_b16 %1, %8 offset:4096\n\tds_read_b64_tr_b16 %2, %8 offset:8192\n\t"
            "ds_read_b64_tr_b16 %3, %8 offset:12288\n\tds_read_b64_tr_b16 %4, %8 offset:16384\n\tds_read_b64_tr_b16 %5, %8 offset:20480\n\t"
            "ds_read_b64_tr_b16 %6, %8 offset:24576\n\tds_read_b64_tr_b16 %7, %8 offset:28672\n\ts_waitcnt lgkmcnt(0)"
            : "=&v"(vf[0]), "=&v"(vf[1]), "=&v"(vf[2]), "=&v"(vf[3]), "=&v"(vf[4]), "=&v"(vf[5]), "=&v"(vf[6]), "=&v"(vf[7])
            : "v"(va) : "memory");
      } else {
        __builtin_amdgcn_s_sleep(D_E1);
        const u32 off[1] = {xl + X_E1 + (u32)(g * 64 + (lane >> 5) * 32 + wave * 4) * 128 + (lane & 31) * 16};
        v4u v[1];
        const int n = gather<1>(rs, off, tag, v, dead);
        STAMPN(18, n);
        float pA = __uint_as_float(v[0].x), pB = __uint_as_float(v[0].z);
        pA = tadd32(pA, pA); pB = tadd32(pB, pB);
        if (lane < 32) *(LAS u32*)(L + L_QS + (wave * 32 + lane) * 4) = pack_bf16(pA, pB);
        lds_barrier();
      }
      v4f sc = {0.f, 0.f, 0.f, 0.f};
#pragma unroll
      for (int s = 0; s < 4; ++s) {
        const v4u qf = *(const LAS v4u*)(L + L_QS + hq * 256 + s * 64 + gq * 16);
        sc = __builtin_amdgcn_mfma_f32_16x16x32_bf16(__builtin_bit_cast(v8bf, kf[s]), __builtin_bit_cast(v8bf, qf), sc, 0, 0, 0);
      }
      constexpr float C2 = 0.08838834764831845f * 1.4426950408889634f;
      float m = fmaxf(fmaxf(sc[0], sc[1]), fmaxf(sc[2], sc[3])) * C2;
      m = tmax16(m, m); m = tmax32(m, m);
      const float p0 = __builtin_amdgcn_exp2f(sc[0] * C2 - m), p1 = __builtin_amdgcn_exp2f(sc[1] * C2 - m);
      const float p2 = __builtin_amdgcn_exp2f(sc[2] * C2 - m), p3 = __builtin_amdgcn_exp2f(sc[3] * C2 - m);
      const u32 pa = pack_bf16(p0, p1), pb = pack_bf16(p2, p3);
      if (kr < 4) *(LAS v2u*)(L + L_P + wave * 128 + hq * 32 + gq * 8) = (v2u){pa, pb};
      float ls = (bf_lo(pa) + bf_hi(pa)) + (bf_lo(pb) + bf_hi(pb));
      ls = tadd16(ls, ls); ls = tadd32(ls, ls);
      if (lane < 4) *(LAS v2u*)(L + L_ML + hq * 64 + wave * 8) = (v2u){__float_as_uint(m), __float_as_uint(ls)};
      lds_barrier();
      v4f oacc[8];
#pragma unroll
      for (int t = 0; t < 8; ++t) {
        const v2u pf = *(const LAS v2u*)(L + L_P + t * 128 + hq * 32 + gq * 8);
        oacc[t] = __builtin_amdgcn_mfma_f32_16x16x16bf16_1k(__builtin_bit_cast(v4s, vf[t]), __builtin_bit_cast(v4s, pf), (v4f){0.f, 0.f, 0.f, 0.f}, 0, 0, 0);
      }
      float mw[8], lw[8];
#pragma unroll
      for (int t = 0; t < 4; ++t) {
        const v4u q4 = *(const LAS v4u*)(L + L_ML + hq * 64 + t * 16);
        mw[2 * t] = __uint_as_float(q4.x); lw[2 * t] = __uint_as_float(q4.y); mw[2 * t + 1] = __uint_as_float(q4.z); lw[2 * t + 1] = __uint_as_float(q4.w);
      }
      float M = mw[0];
#pragma unroll
      for (int t = 1; t < 8; ++t) M = fmaxf(M, mw[t]);
      float Lsum = 0.f; v4f o = {0.f, 0.f, 0.f, 0.f};
#pragma unroll
      for (int t = 0; t < 8; ++t) { const float e = __builtin_amdgcn_exp2f(mw[t] - M); Lsum = fmaf(e, lw[t], Lsum); o += oacc[t] * e; }
      const u32 pbase = xl + X_E2 + (u32)((g * 32 + j) * 17) * 128;
      if (kr < 4) {
        const v4u d = {pack_bf16(o[0], o[1]), tag, pack_bf16(o[2], o[3]), tag};
        __builtin_amdgcn_raw_buffer_store_b128(d, rs, pbase + (hq * 4 + (wave >> 1)) * 128 + ((wave & 1) * 4 + gq) * 16, 0, 16);
      }
      if (wave == 0 && lane < 4) {
        const v4u d = {__float_as_uint(M), tag, __float_as_uint(Lsum), tag};
        __builtin_amdgcn_raw_buffer_store_b128(d, rs, pbase + 16 * 128 + hq * 16, 0, 16);
      }
    }
    STAMP(5 + layer * 14);

    if (wave == 0 && (isC || layer != 0)) {
      __builtin_amdgcn_s_sleep(D_E2);
      u32 off[8];
#pragma unroll
      for (int i = 0; i < 4; ++i) {
        const u32 pb = xl + X_E2 + (u32)((g * 32 + 8 * i + (lane >> 3)) * 17) * 128;
        off[i] = pb + cidx * 128 + (lane & 7) * 16;
        off[4 + i] = pb + 16 * 128 + (cidx >> 2) * 16;
      }
      v4u v[8];
      const int n = gather<8>(rs, off, tag, v, dead);
      STAMPN(6 + layer * 14, n);
      float M = fmaxf(fmaxf(__uint_as_float(v[4].x), __uint_as_float(v[5].x)), fmaxf(__uint_as_float(v[6].x), __uint_as_float(v[7].x)));
      M = fmaxf(M, dppf<ROR8>(M)); M = tmax16(M, M); M = tmax32(M, M);
      float a0 = 0.f, a1 = 0.f, a2 = 0.f, a3 = 0.f, Ls = 0.f;
#pragma unroll
      for (int i = 0; i < 4; ++i) {
        const float e = __builtin_amdgcn_exp2f(__uint_as_float(v[4 + i].x) - M);
        Ls = fmaf(e, __uint_as_float(v[4 + i].z), Ls);
        a0 = fmaf(e, bf_lo(v[i].x), a0); a1 = fmaf(e, bf_hi(v[i].x), a1); a2 = fmaf(e, bf_lo(v[i].z), a2); a3 = fmaf(e, bf_hi(v[i].z), a3);
      }
      a0 += dppf<ROR8>(a0); a1 += dppf<ROR8>(a1); a2 += dppf<ROR8>(a2); a3 += dppf<ROR8>(a3); Ls += dppf<ROR8>(Ls);
      a0 = tadd16(a0, a0); a1 = tadd16(a1, a1); a2 = tadd16(a2, a2); a3 = tadd16(a3, a3); Ls = tadd16(Ls, Ls);
      a0 = tadd32(a0, a0); a1 = tadd32(a1, a1); a2 = tadd32(a2, a2); a3 = tadd32(a3, a3); Ls = tadd32(Ls, Ls);
      const float inv = __builtin_amdgcn_rcpf(Ls);
      if (isC && lane < 8) {
        const v4u d = {pack_bf16(a0 * inv, a1 * inv), tag, pack_bf16(a2 * inv, a3 * inv), tag};
        __builtin_amdgcn_raw_buffer_store_b128(d, rs, xl + X_E3 + (u32)(g * 16 + cidx) * 128 + lane * 16, 0, 16);
      }
      STAMP(7 + layer * 14);
    }
    if (layer == 0) {
      if (isD) { lds_barrier(); load_role_weights(); }
      else { asm volatile("s_waitcnt vmcnt(0)" ::: "memory"); lds_barrier(); }
    }

#pragma unroll
    for (int ph = 0; ph < 2; ++ph) {
      {
        if (ph == 1 && layer == 0 && isD) { asm volatile("s_waitcnt vmcnt(0)" ::: "memory"); lds_barrier(); }
        u32 wr[2][16];
        {
          v4u r[4][2];
#pragma unroll
          for (int i = 0; i < 4; ++i) {
            r[i][0] = *(const LAS v4u*)(L + L_WR + ((wave * 4 + i) * 64 + lane) * 32);
            r[i][1] = *(const LAS v4u*)(L + L_WR + ((wave * 4 + i) * 64 + lane) * 32 + 16);
          }
#pragma unroll
          for (int p = 0; p < 2; ++p)
#pragma unroll
            for (int hh = 0; hh < 2; ++hh)
#pragma unroll
              for (int c = 0; c < 4; ++c) {
                wr[p][hh * 8 + 2 * c] = perm_lo(r[2 * p][hh][c], r[2 * p + 1][hh][c]);
                wr[p][hh * 8 + 2 * c + 1] = perm_hi(r[2 * p][hh][c], r[2 * p + 1][hh][c]);
              }
        }
        lds_barrier();
        float part = 0.f;
        if (ph == 0 || wave < 7) {
          if (ph == 0) __builtin_amdgcn_s_sleep(D_E3); else __builtin_amdgcn_s_sleep(D_E5);
          const u32 off[1] = {xl + (ph == 0 ? X_E3 + (u32)okh * 64 * 128 : X_E5) + (u32)wave * 1024 + lane * 16};
          v4u v[1];
          const int n = gather<1>(rs, off, tag, v, dead);
          STAMPN(8 + ph * 4 + layer * 14, n);
          float ac[16];
#pragma unroll
          for (int c = 0; c < 16; ++c) ac[c] = dot2(v[0].z, wr[1][c], dot2(v[0].x, wr[0][c], 0.f));
          float t8[8], t4[4], t2[2];
#pragma unroll
          for (int c = 0; c < 8; ++c) t8[c] = tadd32(ac[c], ac[c + 8]);
#pragma unroll
          for (int c = 0; c < 4; ++c) t4[c] = tadd16(t8[c], t8[c + 4]);
#pragma unroll
          for (int c = 0; c < 2; ++c) { const float lo = t4[c] + dppf<ROR8>(t4[c]), hi = t4[c + 2] + dppf<ROR8>(t4[c + 2]); t2[c] = (lane & 8) ? hi : lo; }
          { const float lo = t2[0] + dppf<XOR7>(t2[0]), hi = t2[1] + dppf<XOR7>(t2[1]); part = (lane & 4) ? hi : lo; }
          part += dppf<XOR2>(part); part += dppf<XOR1>(part);
        }
        if ((lane & 3) == 0) *(LAS float*)(L + L_RED + (lane >> 2) * 32 + wave * 4) = part;
        lds_barrier();
        if (wave == 0) {
          const int c = lane & 15;
          const v4f r0 = *(const LAS v4f*)(L + L_RED + c * 32), r1 = *(const LAS v4f*)(L + L_RED + c * 32 + 16);
          const float sum = ((r0[0] + r0[1]) + (r0[2] + r0[3])) + ((r1[0] + r1[1]) + (r1[2] + r1[3]));
          if (ph == 0) {
            if (isO && lane < 16) {
              const v2u d = {__float_as_uint(sum), tag};
              __builtin_amdgcn_raw_buffer_store_b64(d, rs, xl + X_E4 + (u32)(okh * 32 + ocg) * 128 + lane * 8, 0, 16);
            }
          } else {
            const float bias = bf_lo(*(const LAS u16*)(L + L_B + 64 + c * 2));
            const float xp = bf_lo(*(const LAS u16*)(L + L_XP + (didx * 16 + c) * 2));
            const float r = rbf(xp + rbf(rbf(sum) + bias));
            const float rn = dppf<XOR1>(r);
            const u32 pr = pack_bf16(r, rn);
            if (isD && lane < 16 && !(lane & 1)) {
              if (layer == 0) {
                const v2u d = {pr, tag};
                __builtin_amdgcn_raw_buffer_store_b64(d, rs, xl + X_E6 + (u32)didx * 128 + (lane >> 1) * 8, 0, 16);
              } else {
                *(u32*)(a.out + didx * 16 + lane) = pr;
              }
            }
          }
          STAMP(9 + ph * 4 + layer * 14);
        }
      }
      if (ph == 0) {
        u32 wr[4][4];
        {
          v2u r[8];
#pragma unroll
          for (int i = 0; i < 8; ++i) r[i] = *(const LAS v2u*)(L + L_WR + (i * 64 + lane) * 64 + wave * 8);
#pragma unroll
          for (int p = 0; p < 4; ++p) {
            wr[p][0] = perm_lo(r[2 * p].x, r[2 * p + 1].x); wr[p][1] = perm_hi(r[2 * p].x, r[2 * p + 1].x);
            wr[p][2] = perm_lo(r[2 * p].y, r[2 * p + 1].y); wr[p][3] = perm_hi(r[2 * p].y, r[2 * p + 1].y);
          }
        }
        float cs;
        {
          float c0 = 0.f, c1 = 0.f, c2 = 0.f, c3 = 0.f;
#pragma unroll
          for (int p = 0; p < 4; ++p) { c0 = dot2(wr[p][0], ONES, c0); c1 = dot2(wr[p][1], ONES, c1); c2 = dot2(wr[p][2], ONES, c2); c3 = dot2(wr[p][3], ONES, c3); }
          cs = rowsum16(tadd16(tadd32(c0, c2), tadd32(c1, c3)));
        }
        const float bias = bf_lo(*(const LAS u16*)(L + L_B + (wave * 4 + (lane >> 4)) * 2));
        const u32 xres = *(const LAS u32*)(L + L_XS + (wave * 32 + (lane & 31)) * 4);
        lds_barrier();
        __builtin_amdgcn_s_sleep(D_E4);
        {
          const u32 off[1] = {xl + X_E4 + (u32)((lane >> 5) * 32 + wave * 4) * 128 + (lane & 31) * 16};
          v4u v[1];
          const int n = gather<1>(rs, off, tag, v, dead);
          STAMPN(10 + layer * 14, n);
          float pA = __uint_as_float(v[0].x), pB = __uint_as_float(v[0].z);
          pA = tadd32(pA, pA); pB = tadd32(pB, pB);
          const u32 at = pack_bf16(pA, pB);
          const u32 xp = pack_bf16(bf_lo(xres) + bf_lo(at), bf_hi(xres) + bf_hi(at));
          if (lane < 32) *(LAS u32*)(L + L_XP + (wave * 32 + lane) * 4) = xp;
        }
        lds_barrier();
        const v4u t = *(const LAS v4u*)(L + L_XP + lane * 16);
        const u32 xv[4] = {t.x, t.y, t.z, t.w};
        float a0 = 0.f, a1 = 0.f, a2 = 0.f, a3 = 0.f, s1 = 0.f, s2 = 0.f;
#pragma unroll
        for (int p = 0; p < 4; ++p) {
          a0 = dot2(xv[p], wr[p][0], a0); a1 = dot2(xv[p], wr[p][1], a1); a2 = dot2(xv[p], wr[p][2], a2); a3 = dot2(xv[p], wr[p][3], a3);
          s1 = dot2(xv[p], ONES, s1); s2 = dot2(xv[p], xv[p], s2);
        }
        const float acc = rowsum16(tadd16(tadd32(a0, a2), tadd32(a1, a3)));
        float ss = tadd32(s1, s2); ss = tadd16(ss, ss); ss = rowsum16(ss);
        const float mean = rdlane(ss, 0) * (1.f / 512.f);
        const float var = fmaxf(rdlane(ss, 32) * (1.f / 512.f) - mean * mean, 0.f);
        const float rstd = __builtin_amdgcn_rsqf(var + 1e-5f);
        const float z = rbf(rbf((acc - mean * cs) * rstd) + bias);
        const float u2 = z * (1.5957691216057308f + 0.07135481627260025f * z * z);
        const float y = z * __builtin_amdgcn_rcpf(1.f + __builtin_amdgcn_exp2f(-1.4426950408889634f * u2));
        const float y0 = rdlane(y, 0), y1 = rdlane(y, 16), y2 = rdlane(y, 32), y3 = rdlane(y, 48);
        if (isH && lane == 0) {
          const v4u d = {pack_bf16(y0, y1), tag, pack_bf16(y2, y3), tag};
          __builtin_amdgcn_raw_buffer_store_b128(d, rs, xl + X_E5 + (u32)hidx * 128 + wave * 16, 0, 16);
        }
        STAMP(11 + layer * 14);
      }
    }
    lds_barrier();
  }
  if (b == 0 && tid == 0) __hip_atomic_store((u32*)a.ws, tag, __ATOMIC_RELAXED, __HIP_MEMORY_SCOPE_AGENT);
}

}

extern "C" void kernel_launch(void* const* d_in, const int* in_sizes, int n_in, void* d_out, int out_size, void* d_ws,
                              size_t ws_size, hipStream_t stream) {
  Args a;
  a.x = (const u16*)d_in[0]; a.kc = (const u16*)d_in[1]; a.vc = (const u16*)d_in[2]; a.wq = (const u16*)d_in[3];
  a.wo = (const u16*)d_in[4]; a.w1 = (const u16*)d_in[6]; a.b1 = (const u16*)d_in[7]; a.w2 = (const u16*)d_in[8];
  a.b2 = (const u16*)d_in[9]; a.out = (u16*)d_out; a.ws = (unsigned char*)d_ws;
  fused_decode<<<NWG, NTHR, 0, stream>>>(a);
}
```

```cpp
#include <hip/hip_runtime.h>
#include <stdint.h>

typedef unsigned u32;
typedef unsigned short u16;
typedef unsigned long long u64;
typedef u32 v4u __attribute__((ext_vector_type(4)));
typedef u32 v2u __attribute__((ext_vector_type(2)));
typedef float v4f __attribute__((ext_vector_type(4)));
typedef short v4s __attribute__((ext_vector_type(4)));
typedef __bf16 v8bf __attribute__((ext_vector_type(8)));
typedef __bf16 v2bf __attribute__((ext_vector_type(2)));
#define LAS __attribute__((address_space(3)))

#define STAMPS 1
#include <cstdio>
#include <vector>
#include <algorithm>
#define D_E1 6
#define D_E2 6
#define D_E3 6
#define D_E4 6
#define D_E5 6
#define D_E6 6
#define DMA_POL " nt"

namespace {

constexpr int SK = 65536, WIN = 4096;
constexpr int NWG = 256, NTHR = 512;

constexpr u32 L_K = 0, L_V = 32768, L_WQ = 65536, L_WR = 81920, L_QS = 147456, L_XS = 148480, L_XP = 149504,
              L_P = 150528, L_ML = 151552, L_RED = 151808, L_B = 152320, L_CNT = 153344, L_ST = 153600, L_END = 153600 + 4096;

constexpr u32 X_E1 = 0;
constexpr u32 X_E2 = X_E1 + 512 * 128;
constexpr u32 X_E3 = X_E2 + 4352 * 128;
constexpr u32 X_E4 = X_E3 + 128 * 128;
constexpr u32 X_E5 = X_E4 + 64 * 128;
constexpr u32 X_E6 = X_E5 + 56 * 128;
constexpr u32 X_LAYER = X_E6 + 32 * 128;
constexpr u32 X_REGION = 2u << 20;
constexpr u32 X_NREG = 64;
constexpr u32 X_BASE = 2u << 20;
constexpr u32 X_DBG = X_BASE + X_NREG * X_REGION;
static_assert(2 * X_LAYER <= X_REGION, "region");

constexpr int SPIN_CAP = 1 << 18;

__device__ __forceinline__ void dma16(const void* gsrc, u32 lds_dst) {
  u32 keep;
  asm volatile("s_mov_b32 %0, m0\n\ts_mov_b32 m0, %2\n\ts_nop 0\n\tglobal_load_lds_dwordx4 %1, off" DMA_POL "\n\ts_mov_b32 m0, %0"
               : "=&s"(keep) : "v"(gsrc), "s"(lds_dst) : "memory");
}
__device__ __forceinline__ void lds_barrier() {
  asm volatile("s_waitcnt lgkmcnt(0)\n\ts_barrier" ::: "memory");
}
__device__ __forceinline__ float dot2(u32 a, u32 b, float c) {
  return __builtin_amdgcn_fdot2_f32_bf16(__builtin_bit_cast(v2bf, a), __builtin_bit_cast(v2bf, b), c, false);
}
__device__ __forceinline__ u32 pack_bf16(float lo, float hi) {
  u32 r;
  asm("v_cvt_pk_bf16_f32 %0, %1, %2" : "=v"(r) : "v"(lo), "v"(hi));
  return r;
}
__device__ __forceinline__ float bf_lo(u32 p) { return __uint_as_float(p << 16); }
__device__ __forceinline__ float bf_hi(u32 p) { return __uint_as_float(p & 0xffff0000u); }
__device__ __forceinline__ float rbf(float x) { return bf_lo(pack_bf16(x, x)); }
__device__ __forceinline__ u32 perm_lo(u32 a, u32 b) { return __builtin_amdgcn_perm(b, a, 0x05040100u); }
__device__ __forceinline__ u32 perm_hi(u32 a, u32 b) { return __builtin_amdgcn_perm(b, a, 0x07060302u); }
__device__ __forceinline__ float rdlane(float x, int l) { return __builtin_bit_cast(float, __builtin_amdgcn_readlane(__builtin_bit_cast(int, x), l)); }

template <int CTRL> __device__ __forceinline__ float dppf(float x) {
  return __builtin_bit_cast(float, __builtin_amdgcn_mov_dpp(__builtin_bit_cast(int, x), CTRL, 0xf, 0xf, true));
}
constexpr int XOR1 = 0xB1, XOR2 = 0x4E, XOR7 = 0x141, ROR8 = 0x128, ROR4 = 0x124, ROR2 = 0x122, ROR1 = 0x121;
constexpr u32 ONES = 0x3f803f80u;

__device__ __forceinline__ float tadd32(float a, float b) {
  auto s = __builtin_amdgcn_permlane32_swap(__float_as_uint(a), __float_as_uint(b), false, false);
  return __uint_as_float(s[0]) + __uint_as_float(s[1]);
}
__device__ __forceinline__ float tadd16(float a, float b) {
  auto s = __builtin_amdgcn_permlane16_swap(__float_as_uint(a), __float_as_uint(b), false, false);
  return __uint_as_float(s[0]) + __uint_as_float(s[1]);
}
__device__ __forceinline__ float tmax32(float a, float b) {
  auto s = __builtin_amdgcn_permlane32_swap(__float_as_uint(a), __float_as_uint(b), false, false);
  return fmaxf(__uint_as_float(s[0]), __uint_as_float(s[1]));
}
__device__ __forceinline__ float tmax16(float a, float b) {
  auto s = __builtin_amdgcn_permlane16_swap(__float_as_uint(a), __float_as_uint(b), false, false);
  return fmaxf(__uint_as_float(s[0]), __uint_as_float(s[1]));
}
__device__ __forceinline__ float rowsum16(float x) {
  x += dppf<ROR8>(x); x += dppf<ROR4>(x); x += dppf<ROR2>(x); x += dppf<ROR1>(x);
  return x;
}
__device__ __forceinline__ float reduce8(const float (&a)[8], int lane) {
  float t4[4], t2[2];
#pragma unroll
  for (int c = 0; c < 4; ++c) t4[c] = tadd32(a[c], a[c + 4]);
#pragma unroll
  for (int c = 0; c < 2; ++c) t2[c] = tadd16(t4[c], t4[c + 2]);
  const float lo = t2[0] + dppf<ROR8>(t2[0]), hi = t2[1] + dppf<ROR8>(t2[1]);
  float r = (lane & 8) ? hi : lo;
  r += dppf<XOR7>(r); r += dppf<XOR2>(r); r += dppf<XOR1>(r);
  return r;
}

template <int N>
__device__ __forceinline__ int gather(__amdgpu_buffer_rsrc_t rs, const u32 (&off)[N], u32 tag, v4u (&v)[N], bool& dead) {
  int spin = 0;
  for (;; ++spin) {
    asm volatile("" ::: "memory");
#pragma unroll
    for (int i = 0; i < N; ++i) v[i] = __builtin_amdgcn_raw_buffer_load_b128(rs, off[i], 0, 16);
    bool ok = true;
#pragma unroll
    for (int i = 0; i < N; ++i) ok &= (v[i].y == tag) & (v[i].w == tag);
    if (__all(ok)) break;
    if (dead || spin > SPIN_CAP) { dead = true; break; }
  }
  return spin;
}

struct Args {
  const u16* x; const u16* kc; const u16* vc; const u16* wq; const u16* wo;
  const u16* w1; const u16* b1; const u16* w2; const u16* b2;
  u16* out; unsigned char* ws;
};

#define STAMP(i) do { if (lane == 0) { *(LAS u64*)(L + L_ST + wave * 512 + (i) * 8) = __builtin_amdgcn_s_memrealtime(); } } while (0)
#define STAMPN(i, n) do { if (lane == 0) { *(LAS u64*)(L + L_ST + wave * 512 + (i) * 8) = __builtin_amdgcn_s_memrealtime(); *(LAS u64*)(L + L_ST + wave * 512 + 256 + (i) * 8) = (u64)(n) + 1; } } while (0)

__global__ void __launch_bounds__(NTHR, 2) fused_decode(Args a) {
  __shared__ __attribute__((aligned(16))) unsigned char smem[L_END];
  LAS unsigned char* L = (LAS unsigned char*)smem;
  const u32 lbase = (u32)(size_t)L;
  const int tid = threadIdx.x, lane = tid & 63, wave = __builtin_amdgcn_readfirstlane(tid >> 6);
  const int b = blockIdx.x, g = b & 7, j = b >> 3;
  *(LAS u64*)(L + L_ST + tid * 8) = 0;
  STAMP(0);
  if (lane == 0) *(LAS u64*)(L + L_ST + wave * 512 + 30 * 8) = __builtin_amdgcn_s_memtime();

  const int qkh = j >> 4, qcg = j & 15;
  const bool isC = j < 16, isO = (j >= 16) & (j < 24), isH = (j >= 24) & (j < 31), isD = j < 4;
  const int cidx = j & 15;
  const int okh = (j >> 2) & 1;
  const int ocg = 4 * g + (j & 3);
  const int hidx = g * 7 + (isH ? j - 24 : 0);
  const int didx = 4 * g + (j & 3);

  u32 epoch;
  if (wave < 4) {
#pragma unroll
    for (int t = 0; t < 4; ++t) {
      const int lp = t * 16 + (lane >> 2), row = 256 * qkh + 4 * lp + wave;
      dma16((const char*)a.wq + (size_t)row * 8192 + g * 1024 + qcg * 64 + (lane & 3) * 16, lbase + L_WQ + (wave * 4 + t) * 1024);
    }
    if (wave == 0) dma16((const char*)a.x + lane * 16, lbase + L_XS);
    if (wave == 1) {
      const char* src = (lane & 7) < 4 ? (const char*)a.b1 + hidx * 64 + (lane & 3) * 16 : (const char*)a.b2 + didx * 32 + (lane & 1) * 16;
      dma16(src, lbase + L_B);
    }
    if (tid == 128) *(LAS u32*)(L + L_CNT) = 0u;
  }
  asm volatile("s_waitcnt lgkmcnt(0)\n\ts_load_dword %0, %1, 0x0\n\ts_barrier" : "=s"(epoch) : "s"(a.ws) : "memory");
  if (wave >= 4) {
    const size_t kvoff = ((size_t)g * SK + (SK - WIN) + j * 128) * 256;
#pragma unroll
    for (int t = 0; t < 8; ++t) {
      const int ins = t * 4 + (wave - 4), row = ins * 4 + (lane >> 4), ch = (lane & 15) ^ (row & 15);
      dma16((const char*)a.kc + kvoff + row * 256 + ch * 16, lbase + L_K + ins * 1024);
    }
#pragma unroll
    for (int t = 0; t < 8; ++t) {
      const int ins = t * 4 + (wave - 4), row = ins * 4 + (lane >> 4), ch = (lane & 15) ^ ((row & 7) << 1);
      dma16((const char*)a.vc + kvoff + row * 256 + ch * 16, lbase + L_V + ins * 1024);
    }
  }
  asm volatile("s_waitcnt lgkmcnt(0)" : "+s"(epoch) :: "memory");
  const u32 tag = epoch + 1u;
  const u32 xreg = X_BASE + (epoch % X_NREG) * X_REGION;
  const __amdgpu_buffer_rsrc_t rs = __builtin_amdgcn_make_buffer_rsrc(a.ws + xreg, 0, X_REGION, 0x00020000);

  auto load_role_weights = [&]() {
    if (isO | isD) {
      const char* wb = isO ? (const char*)a.wo + (size_t)okh * 2048 * 1024 + ocg * 32 : (const char*)a.w2 + didx * 32;
      const int nins = isO ? 64 : 56;
#pragma unroll
      for (int t = 0; t < 8; ++t) {
        if (t < 7 || isO) {
          int ins = (j & 3) * (nins >> 2) + t * 8 + wave;
          if (ins >= nins) ins -= nins;
          const int Pl = (ins & 7) * 32 + (lane >> 1), row = 256 * (ins >> 3) + 4 * (Pl & 63) + (Pl >> 6);
          dma16(wb + (size_t)row * 1024 + (lane & 1) * 16, lbase + L_WR + ins * 1024);
        }
      }
    } else if (isH) {
#pragma unroll
      for (int t = 0; t < 4; ++t) {
        const int tt = (t + 2 * (hidx & 1)) & 3;
        const int lp = tt * 16 + (lane >> 2), row = 8 * lp + wave;
        dma16((const char*)a.w1 + (size_t)row * 3584 + hidx * 64 + (lane & 3) * 16, lbase + L_WR + (wave * 4 + tt) * 1024);
      }
    }
  };

  bool dead = false;
  v4u kf[4]; v2u vf[8];
  STAMP(1);

  for (int layer = 0; layer < 2; ++layer) {
    const u32 xl = layer * X_LAYER;
    if (wave < 4) {
      if (layer == 0) {
        asm volatile("s_waitcnt vmcnt(0)" ::: "memory");
        if (lane == 0) __hip_atomic_fetch_add((LAS u32*)(L + L_CNT), 1u, __ATOMIC_RELAXED, __HIP_MEMORY_SCOPE_WORKGROUP);
        while (__hip_atomic_load((LAS u32*)(L + L_CNT), __ATOMIC_RELAXED, __HIP_MEMORY_SCOPE_WORKGROUP) < 4u) {}
        asm volatile("" ::: "memory");
      }
      u32 wr[2][8];
      {
        v4u r[4];
#pragma unroll
        for (int i = 0; i < 4; ++i) r[i] = *(const LAS v4u*)(L + L_WQ + (i * 64 + lane) * 64 + wave * 16);
#pragma unroll
        for (int p = 0; p < 2; ++p)
#pragma unroll
          for (int c = 0; c < 4; ++c) { wr[p][2 * c] = perm_lo(r[2 * p][c], r[2 * p + 1][c]); wr[p][2 * c + 1] = perm_hi(r[2 * p][c], r[2 * p + 1][c]); }
      }
      float cs;
      {
        float c[8];
#pragma unroll
        for (int q = 0; q < 8; ++q) c[q] = dot2(wr[1][q], ONES, dot2(wr[0][q], ONES, 0.f));
        cs = reduce8(c, lane);
      }
      u32 xv[4];
      if (layer == 0) {
        const v2u t0 = *(const LAS v2u*)(L + L_XS + lane * 8), t1 = *(const LAS v2u*)(L + L_XS + 512 + lane * 8);
        xv[0] = t0.x; xv[1] = t0.y; xv[2] = t1.x; xv[3] = t1.y;
        STAMP(2);
      } else {
        __builtin_amdgcn_s_sleep(D_E6);
        const u32 o0 = X_E6 + (lane >> 2) * 128 + (lane & 3) * 16;
        const u32 off[2] = {o0, o0 + 16 * 128};
        v4u v[2];
        const int n = gather<2>(rs, off, tag, v, dead);
        STAMPN(16, n);
        xv[0] = v[0].x; xv[1] = v[0].z; xv[2] = v[1].x; xv[3] = v[1].z;
        if (wave == 0) { *(LAS v2u*)(L + L_XS + lane * 8) = (v2u){xv[0], xv[1]}; *(LAS v2u*)(L + L_XS + 512 + lane * 8) = (v2u){xv[2], xv[3]}; }
      }
      const u32 xa = qkh ? xv[2] : xv[0], xb = qkh ? xv[3] : xv[1];
      float ac[8];
#pragma unroll
      for (int q = 0; q < 8; ++q) ac[q] = dot2(xb, wr[1][q], dot2(xa, wr[0][q], 0.f));
      float s1 = 0.f, s2 = 0.f;
#pragma unroll
      for (int p = 0; p < 4; ++p) { s1 = dot2(xv[p], ONES, s1); s2 = dot2(xv[p], xv[p], s2); }
      const float acc = reduce8(ac, lane);
      float ss = tadd32(s1, s2); ss = tadd16(ss, ss); ss = rowsum16(ss);
      const float mean = rdlane(ss, 0) * (1.f / 512.f);
      const float var = fmaxf(rdlane(ss, 32) * (1.f / 512.f) - mean * mean, 0.f);
      const float rstd = __builtin_amdgcn_rsqf(var + 1e-5f);
      const float y = (acc - mean * cs) * rstd;
      if ((lane & 7) == 0) {
        const v2u d = {__float_as_uint(y), tag};
        __builtin_amdgcn_raw_buffer_store_b64(d, rs, xl + X_E1 + (u32)(g * 32 + j) * 256 + wave * 64 + lane, 0, 16);
      }
      STAMP(3 + layer * 14);
      if (layer == 0) {
        const v4u z = {0u, ~tag, 0u, ~tag};
#pragma unroll
        for (int rep = 0; rep < 2; ++rep) {
          const int pc = tid + rep * 256;
          const int line = pc >> 3, lay = line >= 21, li = lay ? line - 21 : line;
          u32 o; bool act = pc < 336;
          if (li < 17) o = X_E2 + (u32)((g * 32 + j) * 17 + li) * 128;
          else if (li < 19) { o = X_E1 + (u32)((g * 32 + j) * 2 + li - 17) * 128; act &= (lay != 0); }
          else if (li == 19) { o = X_E3 + (u32)(g * 16 + cidx) * 128; act &= isC; }
          else { o = isO ? X_E4 + (u32)(okh * 32 + ocg) * 128 : isH ? X_E5 + (u32)hidx * 128 : X_E6 + (u32)didx * 128; act &= (isO | isH | isD); }
          if (act) __builtin_amdgcn_raw_buffer_store_b128(z, rs, o + lay * X_LAYER + (pc & 7) * 16, 0, 16);
        }
      }
    }
    if (layer != 0) lds_barrier();

    {
      const int gq = lane >> 4, hq = lane & 3, kr = lane & 15;
      if (layer == 0) {
        if (wave < 4) {
          __builtin_amdgcn_s_sleep(8);
          const u32 o0 = xl + X_E1 + (u32)(g * 64 + (lane >> 5) * 32 + wave * 8) * 128 + (lane & 31) * 16;
          const u32 off[2] = {o0, o0 + 512};
          v4u v[2];
          const int n = gather<2>(rs, off, tag, v, dead);
          STAMPN(4, n);
#pragma unroll
          for (int i = 0; i < 2; ++i) {
            float pA = __uint_as_float(v[i].x), pB = __uint_as_float(v[i].z);
            pA = tadd32(pA, pA); pB = tadd32(pB, pB);
            if (lane < 32) *(LAS u32*)(L + L_QS + (wave * 64 + i * 32 + lane) * 4) = pack_bf16(pA, pB);
          }
        } else {
          asm volatile("s_waitcnt vmcnt(0)" ::: "memory");
          STAMP(4);
        }
        lds_barrier();
        if (!isD) load_role_weights();
#pragma unroll
        for (int s = 0; s < 4; ++s) kf[s] = *(const LAS v4u*)(L + L_K + (wave * 16 + kr) * 256 + (((4 * s + gq) ^ kr) * 16));
        const int vq = (lane & 15) >> 2, vp = lane & 3;
        const int row0 = 4 * gq + vq;
        const u32 va = lbase + L_V + row0 * 256 + (((2 * wave + (vp >> 1)) ^ ((row0 & 7) << 1)) * 16) + (vp & 1) * 8;
        asm volatile(
            "ds_read_b64_tr_b16 %0, %8\n\tds_read_b64_tr_b16 %1, %8 offset:4096\n\tds_read_b64_tr_b16 %2, %8 offset:8192\n\t"
            "ds_read_b64_tr_b16 %3, %8 offset:12288\n\tds_read_b64_tr_b16 %4, %8 offset:16384\n\tds_read_b64_tr_b16 %5, %8 offset:20480\n\t"
            "ds_read_b64_tr_b16 %6, %8 offset:24576\n\tds_read_b64_tr_b16 %7, %8 offset:28672\n\ts_waitcnt lgkmcnt(0)"
            : "=&v"(vf[0]), "=&v"(vf[1]), "=&v"(vf[2]), "=&v"(vf[3]), "=&v"(vf[4]), "=&v"(vf[5]), "=&v"(vf[6]), "=&v"(vf[7])
            : "v"(va) : "memory");
      } else {
        __builtin_amdgcn_s_sleep(D_E1);
        const u32 off[1] = {xl + X_E1 + (u32)(g * 64 + (lane >> 5) * 32 + wave * 4) * 128 + (lane & 31) * 16};
        v4u v[1];
        const int n = gather<1>(rs, off, tag, v, dead);
        STAMPN(18, n);
        float pA = __uint_as_float(v[0].x), pB = __uint_as_float(v[0].z);
        pA = tadd32(pA, pA); pB = tadd32(pB, pB);
        if (lane < 32) *(LAS u32*)(L + L_QS + (wave * 32 + lane) * 4) = pack_bf16(pA, pB);
        lds_barrier();
      }
      v4f sc = {0.f, 0.f, 0.f, 0.f};
#pragma unroll
      for (int s = 0; s < 4; ++s) {
        const v4u qf = *(const LAS v4u*)(L + L_QS + hq * 256 + s * 64 + gq * 16);
        sc = __builtin_amdgcn_mfma_f32_16x16x32_bf16(__builtin_bit_cast(v8bf, kf[s]), __builtin_bit_cast(v8bf, qf), sc, 0, 0, 0);
      }
      constexpr float C2 = 0.08838834764831845f * 1.4426950408889634f;
      float m = fmaxf(fmaxf(sc[0], sc[1]), fmaxf(sc[2], sc[3])) * C2;
      m = tmax16(m, m); m = tmax32(m, m);
      const float p0 = __builtin_amdgcn_exp2f(sc[0] * C2 - m), p1 = __builtin_amdgcn_exp2f(sc[1] * C2 - m);
      const float p2 = __builtin_amdgcn_exp2f(sc[2] * C2 - m), p3 = __builtin_amdgcn_exp2f(sc[3] * C2 - m);
      const u32 pa = pack_bf16(p0, p1), pb = pack_bf16(p2, p3);
      if (kr < 4) *(LAS v2u*)(L + L_P + wave * 128 + hq * 32 + gq * 8) = (v2u){pa, pb};
      float ls = (bf_lo(pa) + bf_hi(pa)) + (bf_lo(pb) + bf_hi(pb));
      ls = tadd16(ls, ls); ls = tadd32(ls, ls);
      if (lane < 4) *(LAS v2u*)(L + L_ML + hq * 64 + wave * 8) = (v2u){__float_as_uint(m), __float_as_uint(ls)};
      lds_barrier();
      v4f oacc[8];
#pragma unroll
      for (int t = 0; t < 8; ++t) {
        const v2u pf = *(const LAS v2u*)(L + L_P + t * 128 + hq * 32 + gq * 8);
        oacc[t] = __builtin_amdgcn_mfma_f32_16x16x16bf16_1k(__builtin_bit_cast(v4s, vf[t]), __builtin_bit_cast(v4s, pf), (v4f){0.f, 0.f, 0.f, 0.f}, 0, 0, 0);
      }
      float mw[8], lw[8];
#pragma unroll
      for (int t = 0; t < 4; ++t) {
        const v4u q4 = *(const LAS v4u*)(L + L_ML + hq * 64 + t * 16);
        mw[2 * t] = __uint_as_float(q4.x); lw[2 * t] = __uint_as_float(q4.y); mw[2 * t + 1] = __uint_as_float(q4.z); lw[2 * t + 1] = __uint_as_float(q4.w);
      }
      float M = mw[0];
#pragma unroll
      for (int t = 1; t < 8; ++t) M = fmaxf(M, mw[t]);
      float Lsum = 0.f; v4f o = {0.f, 0.f, 0.f, 0.f};
#pragma unroll
      for (int t = 0; t < 8; ++t) { const float e = __builtin_amdgcn_exp2f(mw[t] - M); Lsum = fmaf(e, lw[t], Lsum); o += oacc[t] * e; }
      const u32 pbase = xl + X_E2 + (u32)((g * 32 + j) * 17) * 128;
      if (kr < 4) {
        const v4u d = {pack_bf16(o[0], o[1]), tag, pack_bf16(o[2], o[3]), tag};
        __builtin_amdgcn_raw_buffer_store_b128(d, rs, pbase + (hq * 4 + (wave >> 1)) * 128 + ((wave & 1) * 4 + gq) * 16, 0, 16);
      }
      if (wave == 0 && lane < 4) {
        const v4u d = {__float_as_uint(M), tag, __float_as_uint(Lsum), tag};
        __builtin_amdgcn_raw_buffer_store_b128(d, rs, pbase + 16 * 128 + hq * 16, 0, 16);
      }
    }
    STAMP(5 + layer * 14);

    if (wave == 0 && (isC || layer != 0)) {
      __builtin_amdgcn_s_sleep(D_E2);
      u32 off[8];
#pragma unroll
      for (int i = 0; i < 4; ++i) {
        const u32 pb = xl + X_E2 + (u32)((g * 32 + 8 * i + (lane >> 3)) * 17) * 128;
        off[i] = pb + cidx * 128 + (lane & 7) * 16;
        off[4 + i] = pb + 16 * 128 + (cidx >> 2) * 16;
      }
      v4u v[8];
      const int n = gather<8>(rs, off, tag, v, dead);
      STAMPN(6 + layer * 14, n);
      float M = fmaxf(fmaxf(__uint_as_float(v[4].x), __uint_as_float(v[5].x)), fmaxf(__uint_as_float(v[6].x), __uint_as_float(v[7].x)));
      M = fmaxf(M, dppf<ROR8>(M)); M = tmax16(M, M); M = tmax32(M, M);
      float a0 = 0.f, a1 = 0.f, a2 = 0.f, a3 = 0.f, Ls = 0.f;
#pragma unroll
      for (int i = 0; i < 4; ++i) {
        const float e = __builtin_amdgcn_exp2f(__uint_as_float(v[4 + i].x) - M);
        Ls = fmaf(e, __uint_as_float(v[4 + i].z), Ls);
        a0 = fmaf(e, bf_lo(v[i].x), a0); a1 = fmaf(e, bf_hi(v[i].x), a1); a2 = fmaf(e, bf_lo(v[i].z), a2); a3 = fmaf(e, bf_hi(v[i].z), a3);
      }
      a0 += dppf<ROR8>(a0); a1 += dppf<ROR8>(a1); a2 += dppf<ROR8>(a2); a3 += dppf<ROR8>(a3); Ls += dppf<ROR8>(Ls);
      a0 = tadd16(a0, a0); a1 = tadd16(a1, a1); a2 = tadd16(a2, a2); a3 = tadd16(a3, a3); Ls = tadd16(Ls, Ls);
      a0 = tadd32(a0, a0); a1 = tadd32(a1, a1); a2 = tadd32(a2, a2); a3 = tadd32(a3, a3); Ls = tadd32(Ls, Ls);
      const float inv = __builtin_amdgcn_rcpf(Ls);
      if (isC && lane < 8) {
        const v4u d = {pack_bf16(a0 * inv, a1 * inv), tag, pack_bf16(a2 * inv, a3 * inv), tag};
        __builtin_amdgcn_raw_buffer_store_b128(d, rs, xl + X_E3 + (u32)(g * 16 + cidx) * 128 + lane * 16, 0, 16);
      }
      STAMP(7 + layer * 14);
    }
    if (layer == 0) {
      if (isD) { lds_barrier(); load_role_weights(); }
      else { asm volatile("s_waitcnt vmcnt(0)" ::: "memory"); lds_barrier(); }
    }

#pragma unroll
    for (int ph = 0; ph < 2; ++ph) {
      {
        if (ph == 1 && layer == 0 && isD) { asm volatile("s_waitcnt vmcnt(0)" ::: "memory"); lds_barrier(); }
        u32 wr[2][16];
        {
          v4u r[4][2];
#pragma unroll
          for (int i = 0; i < 4; ++i) {
            r[i][0] = *(const LAS v4u*)(L + L_WR + ((wave * 4 + i) * 64 + lane) * 32);
            r[i][1] = *(const LAS v4u*)(L + L_WR + ((wave * 4 + i) * 64 + lane) * 32 + 16);
          }
#pragma unroll
          for (int p = 0; p < 2; ++p)
#pragma unroll
            for (int hh = 0; hh < 2; ++hh)
#pragma unroll
              for (int c = 0; c < 4; ++c) {
                wr[p][hh * 8 + 2 * c] = perm_lo(r[2 * p][hh][c], r[2 * p + 1][hh][c]);
                wr[p][hh * 8 + 2 * c + 1] = perm_hi(r[2 * p][hh][c], r[2 * p + 1][hh][c]);
              }
        }
        lds_barrier();
        float part = 0.f;
        if (ph == 0 || wave < 7) {
          if (ph == 0) __builtin_amdgcn_s_sleep(D_E3); else __builtin_amdgcn_s_sleep(D_E5);
          const u32 off[1] = {xl + (ph == 0 ? X_E3 + (u32)okh * 64 * 128 : X_E5) + (u32)wave * 1024 + lane * 16};
          v4u v[1];
          const int n = gather<1>(rs, off, tag, v, dead);
          STAMPN(8 + ph * 4 + layer * 14, n);
          float ac[16];
#pragma unroll
          for (int c = 0; c < 16; ++c) ac[c] = dot2(v[0].z, wr[1][c], dot2(v[0].x, wr[0][c], 0.f));
          float t8[8], t4[4], t2[2];
#pragma unroll
          for (int c = 0; c < 8; ++c) t8[c] = tadd32(ac[c], ac[c + 8]);
#pragma unroll
          for (int c = 0; c < 4; ++c) t4[c] = tadd16(t8[c], t8[c + 4]);
#pragma unroll
          for (int c = 0; c < 2; ++c) { const float lo = t4[c] + dppf<ROR8>(t4[c]), hi = t4[c + 2] + dppf<ROR8>(t4[c + 2]); t2[c] = (lane & 8) ? hi : lo; }
          { const float lo = t2[0] + dppf<XOR7>(t2[0]), hi = t2[1] + dppf<XOR7>(t2[1]); part = (lane & 4) ? hi : lo; }
          part += dppf<XOR2>(part); part += dppf<XOR1>(part);
        }
        if ((lane & 3) == 0) *(LAS float*)(L + L_RED + (lane >> 2) * 32 + wave * 4) = part;
        lds_barrier();
        if (wave == 0) {
          const int c = lane & 15;
          const v4f r0 = *(const LAS v4f*)(L + L_RED + c * 32), r1 = *(const LAS v4f*)(L + L_RED + c * 32 + 16);
          const float sum = ((r0[0] + r0[1]) + (r0[2] + r0[3])) + ((r1[0] + r1[1]) + (r1[2] + r1[3]));
          if (ph == 0) {
            if (isO && lane < 16) {
              const v2u d = {__float_as_uint(sum), tag};
              __builtin_amdgcn_raw_buffer_store_b64(d, rs, xl + X_E4 + (u32)(okh * 32 + ocg) * 128 + lane * 8, 0, 16);
            }
          } else {
            const float bias = bf_lo(*(const LAS u16*)(L + L_B + 64 + c * 2));
            const float xp = bf_lo(*(const LAS u16*)(L + L_XP + (didx * 16 + c) * 2));
            const float r = rbf(xp + rbf(rbf(sum) + bias));
            const float rn = dppf<XOR1>(r);
            const u32 pr = pack_bf16(r, rn);
            if (isD && lane < 16 && !(lane & 1)) {
              if (layer == 0) {
                const v2u d = {pr, tag};
                __builtin_amdgcn_raw_buffer_store_b64(d, rs, xl + X_E6 + (u32)didx * 128 + (lane >> 1) * 8, 0, 16);
              } else {
                *(u32*)(a.out + didx * 16 + lane) = pr;
              }
            }
          }
          STAMP(9 + ph * 4 + layer * 14);
        }
      }
      if (ph == 0) {
        u32 wr[4][4];
        {
          v2u r[8];
#pragma unroll
          for (int i = 0; i < 8; ++i) r[i] = *(const LAS v2u*)(L + L_WR + (i * 64 + lane) * 64 + wave * 8);
#pragma unroll
          for (int p = 0; p < 4; ++p) {
            wr[p][0] = perm_lo(r[2 * p].x, r[2 * p + 1].x); wr[p][1] = perm_hi(r[2 * p].x, r[2 * p + 1].x);
            wr[p][2] = perm_lo(r[2 * p].y, r[2 * p + 1].y); wr[p][3] = perm_hi(r[2 * p].y, r[2 * p + 1].y);
          }
        }
        float cs;
        {
          float c0 = 0.f, c1 = 0.f, c2 = 0.f, c3 = 0.f;
#pragma unroll
          for (int p = 0; p < 4; ++p) { c0 = dot2(wr[p][0], ONES, c0); c1 = dot2(wr[p][1], ONES, c1); c2 = dot2(wr[p][2], ONES, c2); c3 = dot2(wr[p][3], ONES, c3); }
          cs = rowsum16(tadd16(tadd32(c0, c2), tadd32(c1, c3)));
        }
        const float bias = bf_lo(*(const LAS u16*)(L + L_B + (wave * 4 + (lane >> 4)) * 2));
        const u32 xres = *(const LAS u32*)(L + L_XS + (wave * 32 + (lane & 31)) * 4);
        lds_barrier();
        __builtin_amdgcn_s_sleep(D_E4);
        {
          const u32 off[1] = {xl + X_E4 + (u32)((lane >> 5) * 32 + wave * 4) * 128 + (lane & 31) * 16};
          v4u v[1];
          const int n = gather<1>(rs, off, tag, v, dead);
          STAMPN(10 + layer * 14, n);
          float pA = __uint_as_float(v[0].x), pB = __uint_as_float(v[0].z);
          pA = tadd32(pA, pA); pB = tadd32(pB, pB);
          const u32 at = pack_bf16(pA, pB);
          const u32 xp = pack_bf16(bf_lo(xres) + bf_lo(at), bf_hi(xres) + bf_hi(at));
          if (lane < 32) *(LAS u32*)(L + L_XP + (wave * 32 + lane) * 4) = xp;
        }
        lds_barrier();
        const v4u t = *(const LAS v4u*)(L + L_XP + lane * 16);
        const u32 xv[4] = {t.x, t.y, t.z, t.w};
        float a0 = 0.f, a1 = 0.f, a2 = 0.f, a3 = 0.f, s1 = 0.f, s2 = 0.f;
#pragma unroll
        for (int p = 0; p < 4; ++p) {
          a0 = dot2(xv[p], wr[p][0], a0); a1 = dot2(xv[p], wr[p][1], a1); a2 = dot2(xv[p], wr[p][2], a2); a3 = dot2(xv[p], wr[p][3], a3);
          s1 = dot2(xv[p], ONES, s1); s2 = dot2(xv[p], xv[p], s2);
        }
        const float acc = rowsum16(tadd16(tadd32(a0, a2), tadd32(a1, a3)));
        float ss = tadd32(s1, s2); ss = tadd16(ss, ss); ss = rowsum16(ss);
        const float mean = rdlane(ss, 0) * (1.f / 512.f);
        const float var = fmaxf(rdlane(ss, 32) * (1.f / 512.f) - mean * mean, 0.f);
        const float rstd = __builtin_amdgcn_rsqf(var + 1e-5f);
        const float z = rbf(rbf((acc - mean * cs) * rstd) + bias);
        const float u2 = z * (1.5957691216057308f + 0.07135481627260025f * z * z);
        const float y = z * __builtin_amdgcn_rcpf(1.f + __builtin_amdgcn_exp2f(-1.4426950408889634f * u2));
        const float y0 = rdlane(y, 0), y1 = rdlane(y, 16), y2 = rdlane(y, 32), y3 = rdlane(y, 48);
        if (isH && lane == 0) {
          const v4u d = {pack_bf16(y0, y1), tag, pack_bf16(y2, y3), tag};
          __builtin_amdgcn_raw_buffer_store_b128(d, rs, xl + X_E5 + (u32)hidx * 128 + wave * 16, 0, 16);
        }
        STAMP(11 + layer * 14);
      }
    }
    lds_barrier();
  }
  if (b == 0 && tid == 0) __hip_atomic_store((u32*)a.ws, tag, __ATOMIC_RELAXED, __HIP_MEMORY_SCOPE_AGENT);
  if (lane == 0) { *(LAS u64*)(L + L_ST + wave * 512 + 31 * 8) = __builtin_amdgcn_s_memtime(); *(LAS u64*)(L + L_ST + wave * 512 + 29 * 8) = __builtin_amdgcn_s_memrealtime(); }
  lds_barrier();
  ((u64*)(a.ws + X_DBG))[b * 512 + tid] = *(LAS u64*)(L + L_ST + tid * 8);
}

}

extern "C" void kernel_launch(void* const* d_in, const int* in_sizes, int n_in, void* d_out, int out_size, void* d_ws,
                              size_t ws_size, hipStream_t stream) {
  Args a;
  a.x = (const u16*)d_in[0]; a.kc = (const u16*)d_in[1]; a.vc = (const u16*)d_in[2]; a.wq = (const u16*)d_in[3];
  a.wo = (const u16*)d_in[4]; a.w1 = (const u16*)d_in[6]; a.b1 = (const u16*)d_in[7]; a.w2 = (const u16*)d_in[8];
  a.b2 = (const u16*)d_in[9]; a.out = (u16*)d_out; a.ws = (unsigned char*)d_ws;
  fused_decode<<<NWG, NTHR, 0, stream>>>(a);
  hipStreamCaptureStatus cs; hipStreamIsCapturing(stream, &cs);
  if (cs == hipStreamCaptureStatusNone) {
    for (int i = 0; i < 10; ++i) fprintf(stderr, "in[%d]=%p ", i, d_in[i]);
    fprintf(stderr, "out=%p ws=%p\n", d_out, d_ws);
    for (int it = 0; it < 4; ++it) {
      if (it < 3) hipMemsetAsync((char*)d_ws + (200u << 20), it, 300u << 20, stream);
      hipStreamSynchronize(stream);
      fused_decode<<<NWG, NTHR, 0, stream>>>(a);
      hipStreamSynchronize(stream);
      std::vector<unsigned long long> hs(256 * 512); hipMemcpy(hs.data(), (char*)d_ws + X_DBG, hs.size() * 8, hipMemcpyDeviceToHost);
      unsigned long long t0 = ~0ull; for (int b = 0; b < 256; ++b) for (int w = 0; w < 8; ++w) t0 = std::min(t0, hs[b * 512 + w * 64]);
      if (it >= 2) { fprintf(stderr, "it %d:", it);
      for (int i = 0; i < 30; ++i) { std::vector<double> v; for (int b = 0; b < 256; ++b) for (int w = 0; w < 8; ++w) { unsigned long long t = hs[b * 512 + w * 64 + i]; if (t) v.push_back((t - t0) * 0.01); }
        if (v.empty()) continue; std::sort(v.begin(), v.end()); fprintf(stderr, " [%d]%.1f/%.1f", i, v[v.size() / 2], v.back()); }
      fprintf(stderr, "\n"); }
    }
  }
}
```

```cpp
#define DIAG_B 40
#define DIAG_W 0
#define DIAG_I 19
#define DIAG_J 18
#define DIAG_MUL 10
#define DIAG_DIV 1
#include <hip/hip_runtime.h>
#include <stdint.h>

typedef unsigned u32;
typedef unsigned short u16;
typedef unsigned long long u64;
typedef u32 v4u __attribute__((ext_vector_type(4)));
typedef u32 v2u __attribute__((ext_vector_type(2)));
typedef float v4f __attribute__((ext_vector_type(4)));
typedef short v4s __attribute__((ext_vector_type(4)));
typedef __bf16 v8bf __attribute__((ext_vector_type(8)));
typedef __bf16 v2bf __attribute__((ext_vector_type(2)));
#define LAS __attribute__((address_space(3)))

#define STAMPS 1
#define D_E1 6
#define D_E2 6
#define D_E3 6
#define D_E4 6
#define D_E5 6
#define D_E6 6
#define DMA_POL " nt"

namespace {

constexpr int SK = 65536, WIN = 4096;
constexpr int NWG = 256, NTHR = 512;

constexpr u32 L_K = 0, L_V = 32768, L_WQ = 65536, L_WR = 81920, L_QS = 147456, L_XS = 148480, L_XP = 149504,
              L_P = 150528, L_ML = 151552, L_RED = 151808, L_B = 152320, L_CNT = 153344, L_ST = 153600, L_END = 153600 + 4096;

constexpr u32 X_E1 = 0;
constexpr u32 X_E2 = X_E1 + 512 * 128;
constexpr u32 X_E3 = X_E2 + 4352 * 128;
constexpr u32 X_E4 = X_E3 + 128 * 128;
constexpr u32 X_E5 = X_E4 + 64 * 128;
constexpr u32 X_E6 = X_E5 + 56 * 128;
constexpr u32 X_LAYER = X_E6 + 32 * 128;
constexpr u32 X_REGION = 2u << 20;
constexpr u32 X_NREG = 64;
constexpr u32 X_BASE = 2u << 20;
constexpr u32 X_DBG = X_BASE + X_NREG * X_REGION;
static_assert(2 * X_LAYER <= X_REGION, "region");

constexpr int SPIN_CAP = 1 << 18;

__device__ __forceinline__ void dma16(const void* gsrc, u32 lds_dst) {
  u32 keep;
  asm volatile("s_mov_b32 %0, m0\n\ts_mov_b32 m0, %2\n\ts_nop 0\n\tglobal_load_lds_dwordx4 %1, off" DMA_POL "\n\ts_mov_b32 m0, %0"
               : "=&s"(keep) : "v"(gsrc), "s"(lds_dst) : "memory");
}
__device__ __forceinline__ void lds_barrier() {
  asm volatile("s_waitcnt lgkmcnt(0)\n\ts_barrier" ::: "memory");
}
__device__ __forceinline__ float dot2(u32 a, u32 b, float c) {
  return __builtin_amdgcn_fdot2_f32_bf16(__builtin_bit_cast(v2bf, a), __builtin_bit_cast(v2bf, b), c, false);
}
__device__ __forceinline__ u32 pack_bf16(float lo, float hi) {
  u32 r;
  asm("v_cvt_pk_bf16_f32 %0, %1, %2" : "=v"(r) : "v"(lo), "v"(hi));
  return r;
}
__device__ __forceinline__ float bf_lo(u32 p) { return __uint_as_float(p << 16); }
__device__ __forceinline__ float bf_hi(u32 p) { return __uint_as_float(p & 0xffff0000u); }
__device__ __forceinline__ float rbf(float x) { return bf_lo(pack_bf16(x, x)); }
__device__ __forceinline__ u32 perm_lo(u32 a, u32 b) { return __builtin_amdgcn_perm(b, a, 0x05040100u); }
__device__ __forceinline__ u32 perm_hi(u32 a, u32 b) { return __builtin_amdgcn_perm(b, a, 0x07060302u); }
__device__ __forceinline__ float rdlane(float x, int l) { return __builtin_bit_cast(float, __builtin_amdgcn_readlane(__builtin_bit_cast(int, x), l)); }

template <int CTRL> __device__ __forceinline__ float dppf(float x) {
  return __builtin_bit_cast(float, __builtin_amdgcn_mov_dpp(__builtin_bit_cast(int, x), CTRL, 0xf, 0xf, true));
}
constexpr int XOR1 = 0xB1, XOR2 = 0x4E, XOR7 = 0x141, ROR8 = 0x128, ROR4 = 0x124, ROR2 = 0x122, ROR1 = 0x121;
constexpr u32 ONES = 0x3f803f80u;

__device__ __forceinline__ float tadd32(float a, float b) {
  auto s = __builtin_amdgcn_permlane32_swap(__float_as_uint(a), __float_as_uint(b), false, false);
  return __uint_as_float(s[0]) + __uint_as_float(s[1]);
}
__device__ __forceinline__ float tadd16(float a, float b) {
  auto s = __builtin_amdgcn_permlane16_swap(__float_as_uint(a), __float_as_uint(b), false, false);
  return __uint_as_float(s[0]) + __uint_as_float(s[1]);
}
__device__ __forceinline__ float tmax32(float a, float b) {
  auto s = __builtin_amdgcn_permlane32_swap(__float_as_uint(a), __float_as_uint(b), false, false);
  return fmaxf(__uint_as_float(s[0]), __uint_as_float(s[1]));
}
__device__ __forceinline__ float tmax16(float a, float b) {
  auto s = __builtin_amdgcn_permlane16_swap(__float_as_uint(a), __float_as_uint(b), false, false);
  return fmaxf(__uint_as_float(s[0]), __uint_as_float(s[1]));
}
__device__ __forceinline__ float rowsum16(float x) {
  x += dppf<ROR8>(x); x += dppf<ROR4>(x); x += dppf<ROR2>(x); x += dppf<ROR1>(x);
  return x;
}
__device__ __forceinline__ float reduce8(const float (&a)[8], int lane) {
  float t4[4], t2[2];
#pragma unroll
  for (int c = 0; c < 4; ++c) t4[c] = tadd32(a[c], a[c + 4]);
#pragma unroll
  for (int c = 0; c < 2; ++c) t2[c] = tadd16(t4[c], t4[c + 2]);
  const float lo = t2[0] + dppf<ROR8>(t2[0]), hi = t2[1] + dppf<ROR8>(t2[1]);
  float r = (lane & 8) ? hi : lo;
  r += dppf<XOR7>(r); r += dppf<XOR2>(r); r += dppf<XOR1>(r);
  return r;
}

template <int N>
__device__ __forceinline__ int gather(__amdgpu_buffer_rsrc_t rs, const u32 (&off)[N], u32 tag, v4u (&v)[N], bool& dead) {
  int spin = 0;
  for (;; ++spin) {
    asm volatile("" ::: "memory");
#pragma unroll
    for (int i = 0; i < N; ++i) v[i] = __builtin_amdgcn_raw_buffer_load_b128(rs, off[i], 0, 16);
    bool ok = true;
#pragma unroll
    for (int i = 0; i < N; ++i) ok &= (v[i].y == tag) & (v[i].w == tag);
    if (__all(ok)) break;
    if (dead || spin > SPIN_CAP) { dead = true; break; }
  }
  return spin;
}

struct Args {
  const u16* x; const u16* kc; const u16* vc; const u16* wq; const u16* wo;
  const u16* w1; const u16* b1; const u16* w2; const u16* b2;
  u16* out; unsigned char* ws;
};

#define STAMP(i) do { if (lane == 0) { *(LAS u64*)(L + L_ST + wave * 512 + (i) * 8) = __builtin_amdgcn_s_memrealtime(); } } while (0)
#define STAMPN(i, n) do { if (lane == 0) { *(LAS u64*)(L + L_ST + wave * 512 + (i) * 8) = __builtin_amdgcn_s_memrealtime(); *(LAS u64*)(L + L_ST + wave * 512 + 256 + (i) * 8) = (u64)(n) + 1; } } while (0)

__global__ void __launch_bounds__(NTHR, 2) fused_decode(Args a) {
  __shared__ __attribute__((aligned(16))) unsigned char smem[L_END];
  LAS unsigned char* L = (LAS unsigned char*)smem;
  const u32 lbase = (u32)(size_t)L;
  const int tid = threadIdx.x, lane = tid & 63, wave = __builtin_amdgcn_readfirstlane(tid >> 6);
  const int b = blockIdx.x, g = b & 7, j = b >> 3;
  *(LAS u64*)(L + L_ST + tid * 8) = 0;
  STAMP(0);
  if (lane == 0) *(LAS u64*)(L + L_ST + wave * 512 + 30 * 8) = __builtin_amdgcn_s_memtime();

  const int qkh = j >> 4, qcg = j & 15;
  const bool isC = j < 16, isO = (j >= 16) & (j < 24), isH = (j >= 24) & (j < 31), isD = j < 4;
  const int cidx = j & 15;
  const int okh = (j >> 2) & 1;
  const int ocg = 4 * g + (j & 3);
  const int hidx = g * 7 + (isH ? j - 24 : 0);
  const int didx = 4 * g + (j & 3);

  u32 epoch;
  if (wave < 4) {
#pragma unroll
    for (int t = 0; t < 4; ++t) {
      const int lp = t * 16 + (lane >> 2), row = 256 * qkh + 4 * lp + wave;
      dma16((const char*)a.wq + (size_t)row * 8192 + g * 1024 + qcg * 64 + (lane & 3) * 16, lbase + L_WQ + (wave * 4 + t) * 1024);
    }
    if (wave == 0) dma16((const char*)a.x + lane * 16, lbase + L_XS);
    if (wave == 1) {
      const char* src = (lane & 7) < 4 ? (const char*)a.b1 + hidx * 64 + (lane & 3) * 16 : (const char*)a.b2 + didx * 32 + (lane & 1) * 16;
      dma16(src, lbase + L_B);
    }
    if (tid == 128) *(LAS u32*)(L + L_CNT) = 0u;
  }
  asm volatile("s_waitcnt lgkmcnt(0)\n\ts_load_dword %0, %1, 0x0\n\ts_barrier" : "=s"(epoch) : "s"(a.ws) : "memory");
  if (wave >= 4) {
    const size_t kvoff = ((size_t)g * SK + (SK - WIN) + j * 128) * 256;
#pragma unroll
    for (int t = 0; t < 8; ++t) {
      const int ins = t * 4 + (wave - 4), row = ins * 4 + (lane >> 4), ch = (lane & 15) ^ (row & 15);
      dma16((const char*)a.kc + kvoff + row * 256 + ch * 16, lbase + L_K + ins * 1024);
    }
#pragma unroll
    for (int t = 0; t < 8; ++t) {
      const int ins = t * 4 + (wave - 4), row = ins * 4 + (lane >> 4), ch = (lane & 15) ^ ((row & 7) << 1);
      dma16((const char*)a.vc + kvoff + row * 256 + ch * 16, lbase + L_V + ins * 1024);
    }
  }
  asm volatile("s_waitcnt lgkmcnt(0)" : "+s"(epoch) :: "memory");
  const u32 tag = epoch + 1u;
  const u32 xreg = X_BASE + (epoch % X_NREG) * X_REGION;
  const __amdgpu_buffer_rsrc_t rs = __builtin_amdgcn_make_buffer_rsrc(a.ws + xreg, 0, X_REGION, 0x00020000);

  auto load_role_weights = [&]() {
    if (isO | isD) {
      const char* wb = isO ? (const char*)a.wo + (size_t)okh * 2048 * 1024 + ocg * 32 : (const char*)a.w2 + didx * 32;
      const int nins = isO ? 64 : 56;
#pragma unroll
      for (int t = 0; t < 8; ++t) {
        if (t < 7 || isO) {
          int ins = (j & 3) * (nins >> 2) + t * 8 + wave;
          if (ins >= nins) ins -= nins;
          const int Pl = (ins & 7) * 32 + (lane >> 1), row = 256 * (ins >> 3) + 4 * (Pl & 63) + (Pl >> 6);
          dma16(wb + (size_t)row * 1024 + (lane & 1) * 16, lbase + L_WR + ins * 1024);
        }
      }
    } else if (isH) {
#pragma unroll
      for (int t = 0; t < 4; ++t) {
        const int tt = (t + 2 * (hidx & 1)) & 3;
        const int lp = tt * 16 + (lane >> 2), row = 8 * lp + wave;
        dma16((const char*)a.w1 + (size_t)row * 3584 + hidx * 64 + (lane & 3) * 16, lbase + L_WR + (wave * 4 + tt) * 1024);
      }
    }
  };

  bool dead = false;
  v4u kf[4]; v2u vf[8];
  STAMP(1);

  for (int layer = 0; layer < 2; ++layer) {
    const u32 xl = layer * X_LAYER;
    if (wave < 4) {
      if (layer == 0) {
        asm volatile("s_waitcnt vmcnt(0)" ::: "memory");
        if (lane == 0) __hip_atomic_fetch_add((LAS u32*)(L + L_CNT), 1u, __ATOMIC_RELAXED, __HIP_MEMORY_SCOPE_WORKGROUP);
        while (__hip_atomic_load((LAS u32*)(L + L_CNT), __ATOMIC_RELAXED, __HIP_MEMORY_SCOPE_WORKGROUP) < 4u) {}
        asm volatile("" ::: "memory");
      }
      u32 wr[2][8];
      {
        v4u r[4];
#pragma unroll
        for (int i = 0; i < 4; ++i) r[i] = *(const LAS v4u*)(L + L_WQ + (i * 64 + lane) * 64 + wave * 16);
#pragma unroll
        for (int p = 0; p < 2; ++p)
#pragma unroll
          for (int c = 0; c < 4; ++c) { wr[p][2 * c] = perm_lo(r[2 * p][c], r[2 * p + 1][c]); wr[p][2 * c + 1] = perm_hi(r[2 * p][c], r[2 * p + 1][c]); }
      }
      float cs;
      {
        float c[8];
#pragma unroll
        for (int q = 0; q < 8; ++q) c[q] = dot2(wr[1][q], ONES, dot2(wr[0][q], ONES, 0.f));
        cs = reduce8(c, lane);
      }
      u32 xv[4];
      if (layer == 0) {
        const v2u t0 = *(const LAS v2u*)(L + L_XS + lane * 8), t1 = *(const LAS v2u*)(L + L_XS + 512 + lane * 8);
        xv[0] = t0.x; xv[1] = t0.y; xv[2] = t1.x; xv[3] = t1.y;
        STAMP(2);
      } else {
        __builtin_amdgcn_s_sleep(D_E6);
        const u32 o0 = X_E6 + (lane >> 2) * 128 + (lane & 3) * 16;
        const u32 off[2] = {o0, o0 + 16 * 128};
        v4u v[2];
        const int n = gather<2>(rs, off, tag, v, dead);
        STAMPN(16, n);
        xv[0] = v[0].x; xv[1] = v[0].z; xv[2] = v[1].x; xv[3] = v[1].z;
        if (wave == 0) { *(LAS v2u*)(L + L_XS + lane * 8) = (v2u){xv[0], xv[1]}; *(LAS v2u*)(L + L_XS + 512 + lane * 8) = (v2u){xv[2], xv[3]}; }
      }
      const u32 xa = qkh ? xv[2] : xv[0], xb = qkh ? xv[3] : xv[1];
      float ac[8];
#pragma unroll
      for (int q = 0; q < 8; ++q) ac[q] = dot2(xb, wr[1][q], dot2(xa, wr[0][q], 0.f));
      float s1 = 0.f, s2 = 0.f;
#pragma unroll
      for (int p = 0; p < 4; ++p) { s1 = dot2(xv[p], ONES, s1); s2 = dot2(xv[p], xv[p], s2); }
      const float acc = reduce8(ac, lane);
      float ss = tadd32(s1, s2); ss = tadd16(ss, ss); ss = rowsum16(ss);
      const float mean = rdlane(ss, 0) * (1.f / 512.f);
      const float var = fmaxf(rdlane(ss, 32) * (1.f / 512.f) - mean * mean, 0.f);
      const float rstd = __builtin_amdgcn_rsqf(var + 1e-5f);
      const float y = (acc - mean * cs) * rstd;
      if ((lane & 7) == 0) {
        const v2u d = {__float_as_uint(y), tag};
        __builtin_amdgcn_raw_buffer_store_b64(d, rs, xl + X_E1 + (u32)(g * 32 + j) * 256 + wave * 64 + lane, 0, 16);
      }
      STAMP(3 + layer * 14);
      if (layer == 0) {
        const v4u z = {0u, ~tag, 0u, ~tag};
#pragma unroll
        for (int rep = 0; rep < 2; ++rep) {
          const int pc = tid + rep * 256;
          const int line = pc >> 3, lay = line >= 21, li = lay ? line - 21 : line;
          u32 o; bool act = pc < 336;
          if (li < 17) o = X_E2 + (u32)((g * 32 + j) * 17 + li) * 128;
          else if (li < 19) { o = X_E1 + (u32)((g * 32 + j) * 2 + li - 17) * 128; act &= (lay != 0); }
          else if (li == 19) { o = X_E3 + (u32)(g * 16 + cidx) * 128; act &= isC; }
          else { o = isO ? X_E4 + (u32)(okh * 32 + ocg) * 128 : isH ? X_E5 + (u32)hidx * 128 : X_E6 + (u32)didx * 128; act &= (isO | isH | isD); }
          if (act) __builtin_amdgcn_raw_buffer_store_b128(z, rs, o + lay * X_LAYER + (pc & 7) * 16, 0, 16);
        }
      }
    }
    if (layer != 0) lds_barrier();

    {
      const int gq = lane >> 4, hq = lane & 3, kr = lane & 15;
      if (layer == 0) {
        if (wave < 4) {
          __builtin_amdgcn_s_sleep(8);
          const u32 o0 = xl + X_E1 + (u32)(g * 64 + (lane >> 5) * 32 + wave * 8) * 128 + (lane & 31) * 16;
          const u32 off[2] = {o0, o0 + 512};
          v4u v[2];
          const int n = gather<2>(rs, off, tag, v, dead);
          STAMPN(4, n);
#pragma unroll
          for (int i = 0; i < 2; ++i) {
            float pA = __uint_as_float(v[i].x), pB = __uint_as_float(v[i].z);
            pA = tadd32(pA, pA); pB = tadd32(pB, pB);
            if (lane < 32) *(LAS u32*)(L + L_QS + (wave * 64 + i * 32 + lane) * 4) = pack_bf16(pA, pB);
          }
        } else {
          asm volatile("s_waitcnt vmcnt(0)" ::: "memory");
          STAMP(4);
        }
        lds_barrier();
        if (!isD) load_role_weights();
#pragma unroll
        for (int s = 0; s < 4; ++s) kf[s] = *(const LAS v4u*)(L + L_K + (wave * 16 + kr) * 256 + (((4 * s + gq) ^ kr) * 16));
        const int vq = (lane & 15) >> 2, vp = lane & 3;
        const int row0 = 4 * gq + vq;
        const u32 va = lbase + L_V + row0 * 256 + (((2 * wave + (vp >> 1)) ^ ((row0 & 7) << 1)) * 16) + (vp & 1) * 8;
        asm volatile(
            "ds_read_b64_tr_b16 %0, %8\n\tds_read_b64_tr_b16 %1, %8 offset:4096\n\tds_read_b64_tr_b16 %2, %8 offset:8192\n\t"
            "ds_read_b64_tr_b16 %3, %8 offset:12288\n\tds_read_b64_tr_b16 %4, %8 offset:16384\n\tds_read_b64_tr_b16 %5, %8 offset:20480\n\t"
            "ds_read_b64_tr_b16 %6, %8 offset:24576\n\tds_read_b64_tr_b16 %7, %8 offset:28672\n\ts_waitcnt lgkmcnt(0)"
            : "=&v"(vf[0]), "=&v"(vf[1]), "=&v"(vf[2]), "=&v"(vf[3]), "=&v"(vf[4]), "=&v"(vf[5]), "=&v"(vf[6]), "=&v"(vf[7])
            : "v"(va) : "memory");
      } else {
        __builtin_amdgcn_s_sleep(D_E1);
        const u32 off[1] = {xl + X_E1 + (u32)(g * 64 + (lane >> 5) * 32 + wave * 4) * 128 + (lane & 31) * 16};
        v4u v[1];
        const int n = gather<1>(rs, off, tag, v, dead);
        STAMPN(18, n);
        float pA = __uint_as_float(v[0].x), pB = __uint_as_float(v[0].z);
        pA = tadd32(pA, pA); pB = tadd32(pB, pB);
        if (lane < 32) *(LAS u32*)(L + L_QS + (wave * 32 + lane) * 4) = pack_bf16(pA, pB);
        lds_barrier();
      }
      v4f sc = {0.f, 0.f, 0.f, 0.f};
#pragma unroll
      for (int s = 0; s < 4; ++s) {
        const v4u qf = *(const LAS v4u*)(L + L_QS + hq * 256 + s * 64 + gq * 16);
        sc = __builtin_amdgcn_mfma_f32_16x16x32_bf16(__builtin_bit_cast(v8bf, kf[s]), __builtin_bit_cast(v8bf, qf), sc, 0, 0, 0);
      }
      constexpr float C2 = 0.08838834764831845f * 1.4426950408889634f;
      float m = fmaxf(fmaxf(sc[0], sc[1]), fmaxf(sc[2], sc[3])) * C2;
      m = tmax16(m, m); m = tmax32(m, m);
      const float p0 = __builtin_amdgcn_exp2f(sc[0] * C2 - m), p1 = __builtin_amdgcn_exp2f(sc[1] * C2 - m);
      const float p2 = __builtin_amdgcn_exp2f(sc[2] * C2 - m), p3 = __builtin_amdgcn_exp2f(sc[3] * C2 - m);
      const u32 pa = pack_bf16(p0, p1), pb = pack_bf16(p2, p3);
      if (kr < 4) *(LAS v2u*)(L + L_P + wave * 128 + hq * 32 + gq * 8) = (v2u){pa, pb};
      float ls = (bf_lo(pa) + bf_hi(pa)) + (bf_lo(pb) + bf_hi(pb));
      ls = tadd16(ls, ls); ls = tadd32(ls, ls);
      if (lane < 4) *(LAS v2u*)(L + L_ML + hq * 64 + wave * 8) = (v2u){__float_as_uint(m), __float_as_uint(ls)};
      lds_barrier();
      v4f oacc[8];
#pragma unroll
      for (int t = 0; t < 8; ++t) {
        const v2u pf = *(const LAS v2u*)(L + L_P + t * 128 + hq * 32 + gq * 8);
        oacc[t] = __builtin_amdgcn_mfma_f32_16x16x16bf16_1k(__builtin_bit_cast(v4s, vf[t]), __builtin_bit_cast(v4s, pf), (v4f){0.f, 0.f, 0.f, 0.f}, 0, 0, 0);
      }
      float mw[8], lw[8];
#pragma unroll
      for (int t = 0; t < 4; ++t) {
        const v4u q4 = *(const LAS v4u*)(L + L_ML + hq * 64 + t * 16);
        mw[2 * t] = __uint_as_float(q4.x); lw[2 * t] = __uint_as_float(q4.y); mw[2 * t + 1] = __uint_as_float(q4.z); lw[2 * t + 1] = __uint_as_float(q4.w);
      }
      float M = mw[0];
#pragma unroll
      for (int t = 1; t < 8; ++t) M = fmaxf(M, mw[t]);
      float Lsum = 0.f; v4f o = {0.f, 0.f, 0.f, 0.f};
#pragma unroll
      for (int t = 0; t < 8; ++t) { const float e = __builtin_amdgcn_exp2f(mw[t] - M); Lsum = fmaf(e, lw[t], Lsum); o += oacc[t] * e; }
      const u32 pbase = xl + X_E2 + (u32)((g * 32 + j) * 17) * 128;
      if (kr < 4) {
        const v4u d = {pack_bf16(o[0], o[1]), tag, pack_bf16(o[2], o[3]), tag};
        __builtin_amdgcn_raw_buffer_store_b128(d, rs, pbase + (hq * 4 + (wave >> 1)) * 128 + ((wave & 1) * 4 + gq) * 16, 0, 16);
      }
      if (wave == 0 && lane < 4) {
        const v4u d = {__float_as_uint(M), tag, __float_as_uint(Lsum), tag};
        __builtin_amdgcn_raw_buffer_store_b128(d, rs, pbase + 16 * 128 + hq * 16, 0, 16);
      }
    }
    STAMP(5 + layer * 14);

    if (wave == 0 && (isC || layer != 0)) {
      __builtin_amdgcn_s_sleep(D_E2);
      u32 off[8];
#pragma unroll
      for (int i = 0; i < 4; ++i) {
        const u32 pb = xl + X_E2 + (u32)((g * 32 + 8 * i + (lane >> 3)) * 17) * 128;
        off[i] = pb + cidx * 128 + (lane & 7) * 16;
        off[4 + i] = pb + 16 * 128 + (cidx >> 2) * 16;
      }
      v4u v[8];
      const int n = gather<8>(rs, off, tag, v, dead);
      STAMPN(6 + layer * 14, n);
      float M = fmaxf(fmaxf(__uint_as_float(v[4].x), __uint_as_float(v[5].x)), fmaxf(__uint_as_float(v[6].x), __uint_as_float(v[7].x)));
      M = fmaxf(M, dppf<ROR8>(M)); M = tmax16(M, M); M = tmax32(M, M);
      float a0 = 0.f, a1 = 0.f, a2 = 0.f, a3 = 0.f, Ls = 0.f;
#pragma unroll
      for (int i = 0; i < 4; ++i) {
        const float e = __builtin_amdgcn_exp2f(__uint_as_float(v[4 + i].x) - M);
        Ls = fmaf(e, __uint_as_float(v[4 + i].z), Ls);
        a0 = fmaf(e, bf_lo(v[i].x), a0); a1 = fmaf(e, bf_hi(v[i].x), a1); a2 = fmaf(e, bf_lo(v[i].z), a2); a3 = fmaf(e, bf_hi(v[i].z), a3);
      }
      a0 += dppf<ROR8>(a0); a1 += dppf<ROR8>(a1); a2 += dppf<ROR8>(a2); a3 += dppf<ROR8>(a3); Ls += dppf<ROR8>(Ls);
      a0 = tadd16(a0, a0); a1 = tadd16(a1, a1); a2 = tadd16(a2, a2); a3 = tadd16(a3, a3); Ls = tadd16(Ls, Ls);
      a0 = tadd32(a0, a0); a1 = tadd32(a1, a1); a2 = tadd32(a2, a2); a3 = tadd32(a3, a3); Ls = tadd32(Ls, Ls);
      const float inv = __builtin_amdgcn_rcpf(Ls);
      if (isC && lane < 8) {
        const v4u d = {pack_bf16(a0 * inv, a1 * inv), tag, pack_bf16(a2 * inv, a3 * inv), tag};
        __builtin_amdgcn_raw_buffer_store_b128(d, rs, xl + X_E3 + (u32)(g * 16 + cidx) * 128 + lane * 16, 0, 16);
      }
      STAMP(7 + layer * 14);
    }
    if (layer == 0) {
      if (isD) { lds_barrier(); load_role_weights(); }
      else { asm volatile("s_waitcnt vmcnt(0)" ::: "memory"); lds_barrier(); }
    }

#pragma unroll
    for (int ph = 0; ph < 2; ++ph) {
      {
        if (ph == 1 && layer == 0 && isD) { asm volatile("s_waitcnt vmcnt(0)" ::: "memory"); lds_barrier(); }
        u32 wr[2][16];
        {
          v4u r[4][2];
#pragma unroll
          for (int i = 0; i < 4; ++i) {
            r[i][0] = *(const LAS v4u*)(L + L_WR + ((wave * 4 + i) * 64 + lane) * 32);
            r[i][1] = *(const LAS v4u*)(L + L_WR + ((wave * 4 + i) * 64 + lane) * 32 + 16);
          }
#pragma unroll
          for (int p = 0; p < 2; ++p)
#pragma unroll
            for (int hh = 0; hh < 2; ++hh)
#pragma unroll
              for (int c = 0; c < 4; ++c) {
                wr[p][hh * 8 + 2 * c] = perm_lo(r[2 * p][hh][c], r[2 * p + 1][hh][c]);
                wr[p][hh * 8 + 2 * c + 1] = perm_hi(r[2 * p][hh][c], r[2 * p + 1][hh][c]);
              }
        }
        lds_barrier();
        float part = 0.f;
        if (ph == 0 || wave < 7) {
          if (ph == 0) __builtin_amdgcn_s_sleep(D_E3); else __builtin_amdgcn_s_sleep(D_E5);
          const u32 off[1] = {xl + (ph == 0 ? X_E3 + (u32)okh * 64 * 128 : X_E5) + (u32)wave * 1024 + lane * 16};
          v4u v[1];
          const int n = gather<1>(rs, off, tag, v, dead);
          STAMPN(8 + ph * 4 + layer * 14, n);
          float ac[16];
#pragma unroll
          for (int c = 0; c < 16; ++c) ac[c] = dot2(v[0].z, wr[1][c], dot2(v[0].x, wr[0][c], 0.f));
          float t8[8], t4[4], t2[2];
#pragma unroll
          for (int c = 0; c < 8; ++c) t8[c] = tadd32(ac[c], ac[c + 8]);
#pragma unroll
          for (int c = 0; c < 4; ++c) t4[c] = tadd16(t8[c], t8[c + 4]);
#pragma unroll
          for (int c = 0; c < 2; ++c) { const float lo = t4[c] + dppf<ROR8>(t4[c]), hi = t4[c + 2] + dppf<ROR8>(t4[c + 2]); t2[c] = (lane & 8) ? hi : lo; }
          { const float lo = t2[0] + dppf<XOR7>(t2[0]), hi = t2[1] + dppf<XOR7>(t2[1]); part = (lane & 4) ? hi : lo; }
          part += dppf<XOR2>(part); part += dppf<XOR1>(part);
        }
        if ((lane & 3) == 0) *(LAS float*)(L + L_RED + (lane >> 2) * 32 + wave * 4) = part;
        lds_barrier();
        if (wave == 0) {
          const int c = lane & 15;
          const v4f r0 = *(const LAS v4f*)(L + L_RED + c * 32), r1 = *(const LAS v4f*)(L + L_RED + c * 32 + 16);
          const float sum = ((r0[0] + r0[1]) + (r0[2] + r0[3])) + ((r1[0] + r1[1]) + (r1[2] + r1[3]));
          if (ph == 0) {
            if (isO && lane < 16) {
              const v2u d = {__float_as_uint(sum), tag};
              __builtin_amdgcn_raw_buffer_store_b64(d, rs, xl + X_E4 + (u32)(okh * 32 + ocg) * 128 + lane * 8, 0, 16);
            }
          } else {
            const float bias = bf_lo(*(const LAS u16*)(L + L_B + 64 + c * 2));
            const float xp = bf_lo(*(const LAS u16*)(L + L_XP + (didx * 16 + c) * 2));
            const float r = rbf(xp + rbf(rbf(sum) + bias));
            const float rn = dppf<XOR1>(r);
            const u32 pr = pack_bf16(r, rn);
            if (isD && lane < 16 && !(lane & 1)) {
              if (layer == 0) {
                const v2u d = {pr, tag};
                __builtin_amdgcn_raw_buffer_store_b64(d, rs, xl + X_E6 + (u32)didx * 128 + (lane >> 1) * 8, 0, 16);
              } else {
                *(u32*)(a.out + didx * 16 + lane) = pr;
              }
            }
          }
          STAMP(9 + ph * 4 + layer * 14);
        }
      }
      if (ph == 0) {
        u32 wr[4][4];
        {
          v2u r[8];
#pragma unroll
          for (int i = 0; i < 8; ++i) r[i] = *(const LAS v2u*)(L + L_WR + (i * 64 + lane) * 64 + wave * 8);
#pragma unroll
          for (int p = 0; p < 4; ++p) {
            wr[p][0] = perm_lo(r[2 * p].x, r[2 * p + 1].x); wr[p][1] = perm_hi(r[2 * p].x, r[2 * p + 1].x);
            wr[p][2] = perm_lo(r[2 * p].y, r[2 * p + 1].y); wr[p][3] = perm_hi(r[2 * p].y, r[2 * p + 1].y);
          }
        }
        float cs;
        {
          float c0 = 0.f, c1 = 0.f, c2 = 0.f, c3 = 0.f;
#pragma unroll
          for (int p = 0; p < 4; ++p) { c0 = dot2(wr[p][0], ONES, c0); c1 = dot2(wr[p][1], ONES, c1); c2 = dot2(wr[p][2], ONES, c2); c3 = dot2(wr[p][3], ONES, c3); }
          cs = rowsum16(tadd16(tadd32(c0, c2), tadd32(c1, c3)));
        }
        const float bias = bf_lo(*(const LAS u16*)(L + L_B + (wave * 4 + (lane >> 4)) * 2));
        const u32 xres = *(const LAS u32*)(L + L_XS + (wave * 32 + (lane & 31)) * 4);
        lds_barrier();
        __builtin_amdgcn_s_sleep(D_E4);
        {
          const u32 off[1] = {xl + X_E4 + (u32)((lane >> 5) * 32 + wave * 4) * 128 + (lane & 31) * 16};
          v4u v[1];
          const int n = gather<1>(rs, off, tag, v, dead);
          STAMPN(10 + layer * 14, n);
          float pA = __uint_as_float(v[0].x), pB = __uint_as_float(v[0].z);
          pA = tadd32(pA, pA); pB = tadd32(pB, pB);
          const u32 at = pack_bf16(pA, pB);
          const u32 xp = pack_bf16(bf_lo(xres) + bf_lo(at), bf_hi(xres) + bf_hi(at));
          if (lane < 32) *(LAS u32*)(L + L_XP + (wave * 32 + lane) * 4) = xp;
        }
        lds_barrier();
        const v4u t = *(const LAS v4u*)(L + L_XP + lane * 16);
        const u32 xv[4] = {t.x, t.y, t.z, t.w};
        float a0 = 0.f, a1 = 0.f, a2 = 0.f, a3 = 0.f, s1 = 0.f, s2 = 0.f;
#pragma unroll
        for (int p = 0; p < 4; ++p) {
          a0 = dot2(xv[p], wr[p][0], a0); a1 = dot2(xv[p], wr[p][1], a1); a2 = dot2(xv[p], wr[p][2], a2); a3 = dot2(xv[p], wr[p][3], a3);
          s1 = dot2(xv[p], ONES, s1); s2 = dot2(xv[p], xv[p], s2);
        }
        const float acc = rowsum16(tadd16(tadd32(a0, a2), tadd32(a1, a3)));
        float ss = tadd32(s1, s2); ss = tadd16(ss, ss); ss = rowsum16(ss);
        const float mean = rdlane(ss, 0) * (1.f / 512.f);
        const float var = fmaxf(rdlane(ss, 32) * (1.f / 512.f) - mean * mean, 0.f);
        const float rstd = __builtin_amdgcn_rsqf(var + 1e-5f);
        const float z = rbf(rbf((acc - mean * cs) * rstd) + bias);
        const float u2 = z * (1.5957691216057308f + 0.07135481627260025f * z * z);
        const float y = z * __builtin_amdgcn_rcpf(1.f + __builtin_amdgcn_exp2f(-1.4426950408889634f * u2));
        const float y0 = rdlane(y, 0), y1 = rdlane(y, 16), y2 = rdlane(y, 32), y3 = rdlane(y, 48);
        if (isH && lane == 0) {
          const v4u d = {pack_bf16(y0, y1), tag, pack_bf16(y2, y3), tag};
          __builtin_amdgcn_raw_buffer_store_b128(d, rs, xl + X_E5 + (u32)hidx * 128 + wave * 16, 0, 16);
        }
        STAMP(11 + layer * 14);
      }
    }
    lds_barrier();
  }
  if (b == 0 && tid == 0) __hip_atomic_store((u32*)a.ws, tag, __ATOMIC_RELAXED, __HIP_MEMORY_SCOPE_AGENT);
  if (lane == 0) { *(LAS u64*)(L + L_ST + wave * 512 + 31 * 8) = __builtin_amdgcn_s_memtime(); *(LAS u64*)(L + L_ST + wave * 512 + 29 * 8) = __builtin_amdgcn_s_memrealtime(); }
  lds_barrier();
  if (b == DIAG_B && wave == 0) {
    const u64 ta = *(LAS u64*)(L + L_ST + DIAG_W * 512 + DIAG_I * 8), tb = *(LAS u64*)(L + L_ST + DIAG_W * 512 + DIAG_J * 8);
    u64 d = (ta - tb) * DIAG_MUL / DIAG_DIV; if (d > 100000) d = 100000;
    const u64 t1 = __builtin_amdgcn_s_memrealtime() + d;
    while (__builtin_amdgcn_s_memrealtime() < t1) {}
  }
  ((u64*)(a.ws + X_DBG))[b * 512 + tid] = *(LAS u64*)(L + L_ST + tid * 8);
}

}

extern "C" void kernel_launch(void* const* d_in, const int* in_sizes, int n_in, void* d_out, int out_size, void* d_ws,
                              size_t ws_size, hipStream_t stream) {
  Args a;
  a.x = (const u16*)d_in[0]; a.kc = (const u16*)d_in[1]; a.vc = (const u16*)d_in[2]; a.wq = (const u16*)d_in[3];
  a.wo = (const u16*)d_in[4]; a.w1 = (const u16*)d_in[6]; a.b1 = (const u16*)d_in[7]; a.w2 = (const u16*)d_in[8];
  a.b2 = (const u16*)d_in[9]; a.out = (u16*)d_out; a.ws = (unsigned char*)d_ws;
  fused_decode<<<NWG, NTHR, 0, stream>>>(a);
}
```

```cpp
#define DIAG_X 3
#define DIAG_OWN 0
#include <hip/hip_runtime.h>
#include <stdint.h>

typedef unsigned u32;
typedef unsigned short u16;
typedef unsigned long long u64;
typedef u32 v4u __attribute__((ext_vector_type(4)));
typedef u32 v2u __attribute__((ext_vector_type(2)));
typedef float v4f __attribute__((ext_vector_type(4)));
typedef short v4s __attribute__((ext_vector_type(4)));
typedef __bf16 v8bf __attribute__((ext_vector_type(8)));
typedef __bf16 v2bf __attribute__((ext_vector_type(2)));
#define LAS __attribute__((address_space(3)))

#define STAMPS 1
#define D_E1 6
#define D_E2 6
#define D_E3 6
#define D_E4 6
#define D_E5 6
#define D_E6 6
#define DMA_POL " nt"

namespace {

constexpr int SK = 65536, WIN = 4096;
constexpr int NWG = 256, NTHR = 512;

constexpr u32 L_K = 0, L_V = 32768, L_WQ = 65536, L_WR = 81920, L_QS = 147456, L_XS = 148480, L_XP = 149504,
              L_P = 150528, L_ML = 151552, L_RED = 151808, L_B = 152320, L_CNT = 153344, L_ST = 153600, L_END = 153600 + 4096;

constexpr u32 X_E1 = 0;
constexpr u32 X_E2 = X_E1 + 512 * 128;
constexpr u32 X_E3 = X_E2 + 4352 * 128;
constexpr u32 X_E4 = X_E3 + 128 * 128;
constexpr u32 X_E5 = X_E4 + 64 * 128;
constexpr u32 X_E6 = X_E5 + 56 * 128;
constexpr u32 X_LAYER = X_E6 + 32 * 128;
constexpr u32 X_REGION = 2u << 20;
constexpr u32 X_NREG = 64;
constexpr u32 X_BASE = 2u << 20;
constexpr u32 X_DBG = X_BASE + X_NREG * X_REGION;
static_assert(2 * X_LAYER <= X_REGION, "region");

constexpr int SPIN_CAP = 1 << 18;

__device__ __forceinline__ void dma16(const void* gsrc, u32 lds_dst) {
  u32 keep;
  asm volatile("s_mov_b32 %0, m0\n\ts_mov_b32 m0, %2\n\ts_nop 0\n\tglobal_load_lds_dwordx4 %1, off" DMA_POL "\n\ts_mov_b32 m0, %0"
               : "=&s"(keep) : "v"(gsrc), "s"(lds_dst) : "memory");
}
__device__ __forceinline__ void lds_barrier() {
  asm volatile("s_waitcnt lgkmcnt(0)\n\ts_barrier" ::: "memory");
}
__device__ __forceinline__ float dot2(u32 a, u32 b, float c) {
  return __builtin_amdgcn_fdot2_f32_bf16(__builtin_bit_cast(v2bf, a), __builtin_bit_cast(v2bf, b), c, false);
}
__device__ __forceinline__ u32 pack_bf16(float lo, float hi) {
  u32 r;
  asm("v_cvt_pk_bf16_f32 %0, %1, %2" : "=v"(r) : "v"(lo), "v"(hi));
  return r;
}
__device__ __forceinline__ float bf_lo(u32 p) { return __uint_as_float(p << 16); }
__device__ __forceinline__ float bf_hi(u32 p) { return __uint_as_float(p & 0xffff0000u); }
__device__ __forceinline__ float rbf(float x) { return bf_lo(pack_bf16(x, x)); }
__device__ __forceinline__ u32 perm_lo(u32 a, u32 b) { return __builtin_amdgcn_perm(b, a, 0x05040100u); }
__device__ __forceinline__ u32 perm_hi(u32 a, u32 b) { return __builtin_amdgcn_perm(b, a, 0x07060302u); }
__device__ __forceinline__ float rdlane(float x, int l) { return __builtin_bit_cast(float, __builtin_amdgcn_readlane(__builtin_bit_cast(int, x), l)); }

template <int CTRL> __device__ __forceinline__ float dppf(float x) {
  return __builtin_bit_cast(float, __builtin_amdgcn_mov_dpp(__builtin_bit_cast(int, x), CTRL, 0xf, 0xf, true));
}
constexpr int XOR1 = 0xB1, XOR2 = 0x4E, XOR7 = 0x141, ROR8 = 0x128, ROR4 = 0x124, ROR2 = 0x122, ROR1 = 0x121;
constexpr u32 ONES = 0x3f803f80u;

__device__ __forceinline__ float tadd32(float a, float b) {
  auto s = __builtin_amdgcn_permlane32_swap(__float_as_uint(a), __float_as_uint(b), false, false);
  return __uint_as_float(s[0]) + __uint_as_float(s[1]);
}
__device__ __forceinline__ float tadd16(float a, float b) {
  auto s = __builtin_amdgcn_permlane16_swap(__float_as_uint(a), __float_as_uint(b), false, false);
  return __uint_as_float(s[0]) + __uint_as_float(s[1]);
}
__device__ __forceinline__ float tmax32(float a, float b) {
  auto s = __builtin_amdgcn_permlane32_swap(__float_as_uint(a), __float_as_uint(b), false, false);
  return fmaxf(__uint_as_float(s[0]), __uint_as_float(s[1]));
}
__device__ __forceinline__ float tmax16(float a, float b) {
  auto s = __builtin_amdgcn_permlane16_swap(__float_as_uint(a), __float_as_uint(b), false, false);
  return fmaxf(__uint_as_float(s[0]), __uint_as_float(s[1]));
}
__device__ __forceinline__ float rowsum16(float x) {
  x += dppf<ROR8>(x); x += dppf<ROR4>(x); x += dppf<ROR2>(x); x += dppf<ROR1>(x);
  return x;
}
__device__ __forceinline__ float reduce8(const float (&a)[8], int lane) {
  float t4[4], t2[2];
#pragma unroll
  for (int c = 0; c < 4; ++c) t4[c] = tadd32(a[c], a[c + 4]);
#pragma unroll
  for (int c = 0; c < 2; ++c) t2[c] = tadd16(t4[c], t4[c + 2]);
  const float lo = t2[0] + dppf<ROR8>(t2[0]), hi = t2[1] + dppf<ROR8>(t2[1]);
  float r = (lane & 8) ? hi : lo;
  r += dppf<XOR7>(r); r += dppf<XOR2>(r); r += dppf<XOR1>(r);
  return r;
}

template <int N>
__device__ __forceinline__ int gather(__amdgpu_buffer_rsrc_t rs, const u32 (&off)[N], u32 tag, v4u (&v)[N], bool& dead) {
  int spin = 0;
  for (;; ++spin) {
    asm volatile("" ::: "memory");
#pragma unroll
    for (int i = 0; i < N; ++i) v[i] = __builtin_amdgcn_raw_buffer_load_b128(rs, off[i], 0, 16);
    bool ok = true;
#pragma unroll
    for (int i = 0; i < N; ++i) ok &= (v[i].y == tag) & (v[i].w == tag);
    if (__all(ok)) break;
    if (dead || spin > SPIN_CAP) { dead = true; break; }
  }
  return spin;
}

struct Args {
  const u16* x; const u16* kc; const u16* vc; const u16* wq; const u16* wo;
  const u16* w1; const u16* b1; const u16* w2; const u16* b2;
  u16* out; unsigned char* ws;
};

#define STAMP(i) do { if (lane == 0) { *(LAS u64*)(L + L_ST + wave * 512 + (i) * 8) = __builtin_amdgcn_s_memrealtime(); } } while (0)
#define STAMPN(i, n) do { if (lane == 0) { *(LAS u64*)(L + L_ST + wave * 512 + (i) * 8) = __builtin_amdgcn_s_memrealtime(); *(LAS u64*)(L + L_ST + wave * 512 + 256 + (i) * 8) = (u64)(n) + 1; } } while (0)

__global__ void __launch_bounds__(NTHR, 2) fused_decode(Args a) {
  __shared__ __attribute__((aligned(16))) unsigned char smem[L_END];
  LAS unsigned char* L = (LAS unsigned char*)smem;
  const u32 lbase = (u32)(size_t)L;
  const int tid = threadIdx.x, lane = tid & 63, wave = __builtin_amdgcn_readfirstlane(tid >> 6);
  const int b = blockIdx.x, g = b & 7, j = b >> 3;
  *(LAS u64*)(L + L_ST + tid * 8) = 0;
  STAMP(0);
  if (lane == 0) *(LAS u64*)(L + L_ST + wave * 512 + 30 * 8) = __builtin_amdgcn_s_memtime();

  const int qkh = j >> 4, qcg = j & 15;
  const bool isC = j < 16, isO = (j >= 16) & (j < 24), isH = (j >= 24) & (j < 31), isD = j < 4;
  const int cidx = j & 15;
  const int okh = (j >> 2) & 1;
  const int ocg = 4 * g + (j & 3);
  const int hidx = g * 7 + (isH ? j - 24 : 0);
  const int didx = 4 * g + (j & 3);

  u32 epoch;
  if (wave < 4) {
#pragma unroll
    for (int t = 0; t < 4; ++t) {
      const int lp = t * 16 + (lane >> 2), row = 256 * qkh + 4 * lp + wave;
      dma16((const char*)a.wq + (size_t)row * 8192 + g * 1024 + qcg * 64 + (lane & 3) * 16, lbase + L_WQ + (wave * 4 + t) * 1024);
    }
    if (wave == 0) dma16((const char*)a.x + lane * 16, lbase + L_XS);
    if (wave == 1) {
      const char* src = (lane & 7) < 4 ? (const char*)a.b1 + hidx * 64 + (lane & 3) * 16 : (const char*)a.b2 + didx * 32 + (lane & 1) * 16;
      dma16(src, lbase + L_B);
    }
    if (tid == 128) *(LAS u32*)(L + L_CNT) = 0u;
  }
  asm volatile("s_waitcnt lgkmcnt(0)\n\ts_load_dword %0, %1, 0x0\n\ts_barrier" : "=s"(epoch) : "s"(a.ws) : "memory");
  if (wave >= 4) {
    const size_t kvoff = ((size_t)g * SK + (SK - WIN) + j * 128) * 256;
#pragma unroll
    for (int t = 0; t < 8; ++t) {
      const int ins = t * 4 + (wave - 4), row = ins * 4 + (lane >> 4), ch = (lane & 15) ^ (row & 15);
      dma16((const char*)a.kc + kvoff + row * 256 + ch * 16, lbase + L_K + ins * 1024);
    }
#pragma unroll
    for (int t = 0; t < 8; ++t) {
      const int ins = t * 4 + (wave - 4), row = ins * 4 + (lane >> 4), ch = (lane & 15) ^ ((row & 7) << 1);
      dma16((const char*)a.vc + kvoff + row * 256 + ch * 16, lbase + L_V + ins * 1024);
    }
  }
  asm volatile("s_waitcnt lgkmcnt(0)" : "+s"(epoch) :: "memory");
  const u32 tag = epoch + 1u;
  const u32 xreg = X_BASE + (epoch % X_NREG) * X_REGION;
  const __amdgpu_buffer_rsrc_t rs = __builtin_amdgcn_make_buffer_rsrc(a.ws + xreg, 0, X_REGION, 0x00020000);

  auto load_role_weights = [&]() {
    if (isO | isD) {
      const char* wb = isO ? (const char*)a.wo + (size_t)okh * 2048 * 1024 + ocg * 32 : (const char*)a.w2 + didx * 32;
      const int nins = isO ? 64 : 56;
#pragma unroll
      for (int t = 0; t < 8; ++t) {
        if (t < 7 || isO) {
          int ins = (j & 3) * (nins >> 2) + t * 8 + wave;
          if (ins >= nins) ins -= nins;
          const int Pl = (ins & 7) * 32 + (lane >> 1), row = 256 * (ins >> 3) + 4 * (Pl & 63) + (Pl >> 6);
          dma16(wb + (size_t)row * 1024 + (lane & 1) * 16, lbase + L_WR + ins * 1024);
        }
      }
    } else if (isH) {
#pragma unroll
      for (int t = 0; t < 4; ++t) {
        const int tt = (t + 2 * (hidx & 1)) & 3;
        const int lp = tt * 16 + (lane >> 2), row = 8 * lp + wave;
        dma16((const char*)a.w1 + (size_t)row * 3584 + hidx * 64 + (lane & 3) * 16, lbase + L_WR + (wave * 4 + tt) * 1024);
      }
    }
  };

  if (tid == 0) { const u64 t00 = *(LAS u64*)(L + L_ST); __hip_atomic_store((u64*)(a.ws + xreg + 0x1c0000) + b, t00, __ATOMIC_RELAXED, __HIP_MEMORY_SCOPE_AGENT); }
  bool dead = false;
  v4u kf[4]; v2u vf[8];
  STAMP(1);

  for (int layer = 0; layer < 2; ++layer) {
    const u32 xl = layer * X_LAYER;
    if (wave < 4) {
      if (layer == 0) {
        asm volatile("s_waitcnt vmcnt(0)" ::: "memory");
        if (lane == 0) __hip_atomic_fetch_add((LAS u32*)(L + L_CNT), 1u, __ATOMIC_RELAXED, __HIP_MEMORY_SCOPE_WORKGROUP);
        while (__hip_atomic_load((LAS u32*)(L + L_CNT), __ATOMIC_RELAXED, __HIP_MEMORY_SCOPE_WORKGROUP) < 4u) {}
        asm volatile("" ::: "memory");
      }
      u32 wr[2][8];
      {
        v4u r[4];
#pragma unroll
        for (int i = 0; i < 4; ++i) r[i] = *(const LAS v4u*)(L + L_WQ + (i * 64 + lane) * 64 + wave * 16);
#pragma unroll
        for (int p = 0; p < 2; ++p)
#pragma unroll
          for (int c = 0; c < 4; ++c) { wr[p][2 * c] = perm_lo(r[2 * p][c], r[2 * p + 1][c]); wr[p][2 * c + 1] = perm_hi(r[2 * p][c], r[2 * p + 1][c]); }
      }
      float cs;
      {
        float c[8];
#pragma unroll
        for (int q = 0; q < 8; ++q) c[q] = dot2(wr[1][q], ONES, dot2(wr[0][q], ONES, 0.f));
        cs = reduce8(c, lane);
      }
      u32 xv[4];
      if (layer == 0) {
        const v2u t0 = *(const LAS v2u*)(L + L_XS + lane * 8), t1 = *(const LAS v2u*)(L + L_XS + 512 + lane * 8);
        xv[0] = t0.x; xv[1] = t0.y; xv[2] = t1.x; xv[3] = t1.y;
        STAMP(2);
      } else {
        __builtin_amdgcn_s_sleep(D_E6);
        const u32 o0 = X_E6 + (lane >> 2) * 128 + (lane & 3) * 16;
        const u32 off[2] = {o0, o0 + 16 * 128};
        v4u v[2];
        const int n = gather<2>(rs, off, tag, v, dead);
        STAMPN(16, n);
        xv[0] = v[0].x; xv[1] = v[0].z; xv[2] = v[1].x; xv[3] = v[1].z;
        if (wave == 0) { *(LAS v2u*)(L + L_XS + lane * 8) = (v2u){xv[0], xv[1]}; *(LAS v2u*)(L + L_XS + 512 + lane * 8) = (v2u){xv[2], xv[3]}; }
      }
      const u32 xa = qkh ? xv[2] : xv[0], xb = qkh ? xv[3] : xv[1];
      float ac[8];
#pragma unroll
      for (int q = 0; q < 8; ++q) ac[q] = dot2(xb, wr[1][q], dot2(xa, wr[0][q], 0.f));
      float s1 = 0.f, s2 = 0.f;
#pragma unroll
      for (int p = 0; p < 4; ++p) { s1 = dot2(xv[p], ONES, s1); s2 = dot2(xv[p], xv[p], s2); }
      const float acc = reduce8(ac, lane);
      float ss = tadd32(s1, s2); ss = tadd16(ss, ss); ss = rowsum16(ss);
      const float mean = rdlane(ss, 0) * (1.f / 512.f);
      const float var = fmaxf(rdlane(ss, 32) * (1.f / 512.f) - mean * mean, 0.f);
      const float rstd = __builtin_amdgcn_rsqf(var + 1e-5f);
      const float y = (acc - mean * cs) * rstd;
      if ((lane & 7) == 0) {
        const v2u d = {__float_as_uint(y), tag};
        __builtin_amdgcn_raw_buffer_store_b64(d, rs, xl + X_E1 + (u32)(g * 32 + j) * 256 + wave * 64 + lane, 0, 16);
      }
      STAMP(3 + layer * 14);
      if (layer == 0) {
        const v4u z = {0u, ~tag, 0u, ~tag};
#pragma unroll
        for (int rep = 0; rep < 2; ++rep) {
          const int pc = tid + rep * 256;
          const int line = pc >> 3, lay = line >= 21, li = lay ? line - 21 : line;
          u32 o; bool act = pc < 336;
          if (li < 17) o = X_E2 + (u32)((g * 32 + j) * 17 + li) * 128;
          else if (li < 19) { o = X_E1 + (u32)((g * 32 + j) * 2 + li - 17) * 128; act &= (lay != 0); }
          else if (li == 19) { o = X_E3 + (u32)(g * 16 + cidx) * 128; act &= isC; }
          else { o = isO ? X_E4 + (u32)(okh * 32 + ocg) * 128 : isH ? X_E5 + (u32)hidx * 128 : X_E6 + (u32)didx * 128; act &= (isO | isH | isD); }
          if (act) __builtin_amdgcn_raw_buffer_store_b128(z, rs, o + lay * X_LAYER + (pc & 7) * 16, 0, 16);
        }
      }
    }
    if (layer != 0) lds_barrier();

    {
      const int gq = lane >> 4, hq = lane & 3, kr = lane & 15;
      if (layer == 0) {
        if (wave < 4) {
          __builtin_amdgcn_s_sleep(8);
          const u32 o0 = xl + X_E1 + (u32)(g * 64 + (lane >> 5) * 32 + wave * 8) * 128 + (lane & 31) * 16;
          const u32 off[2] = {o0, o0 + 512};
          v4u v[2];
          const int n = gather<2>(rs, off, tag, v, dead);
          STAMPN(4, n);
#pragma unroll
          for (int i = 0; i < 2; ++i) {
            float pA = __uint_as_float(v[i].x), pB = __uint_as_float(v[i].z);
            pA = tadd32(pA, pA); pB = tadd32(pB, pB);
            if (lane < 32) *(LAS u32*)(L + L_QS + (wave * 64 + i * 32 + lane) * 4) = pack_bf16(pA, pB);
          }
        } else {
          asm volatile("s_waitcnt vmcnt(0)" ::: "memory");
          STAMP(4);
        }
        lds_barrier();
        if (!isD) load_role_weights();
#pragma unroll
        for (int s = 0; s < 4; ++s) kf[s] = *(const LAS v4u*)(L + L_K + (wave * 16 + kr) * 256 + (((4 * s + gq) ^ kr) * 16));
        const int vq = (lane & 15) >> 2, vp = lane & 3;
        const int row0 = 4 * gq + vq;
        const u32 va = lbase + L_V + row0 * 256 + (((2 * wave + (vp >> 1)) ^ ((row0 & 7) << 1)) * 16) + (vp & 1) * 8;
        asm volatile(
            "ds_read_b64_tr_b16 %0, %8\n\tds_read_b64_tr_b16 %1, %8 offset:4096\n\tds_read_b64_tr_b16 %2, %8 offset:8192\n\t"
            "ds_read_b64_tr_b16 %3, %8 offset:12288\n\tds_read_b64_tr_b16 %4, %8 offset:16384\n\tds_read_b64_tr_b16 %5, %8 offset:20480\n\t"
            "ds_read_b64_tr_b16 %6, %8 offset:24576\n\tds_read_b64_tr_b16 %7, %8 offset:28672\n\ts_waitcnt lgkmcnt(0)"
            : "=&v"(vf[0]), "=&v"(vf[1]), "=&v"(vf[2]), "=&v"(vf[3]), "=&v"(vf[4]), "=&v"(vf[5]), "=&v"(vf[6]), "=&v"(vf[7])
            : "v"(va) : "memory");
      } else {
        __builtin_amdgcn_s_sleep(D_E1);
        const u32 off[1] = {xl + X_E1 + (u32)(g * 64 + (lane >> 5) * 32 + wave * 4) * 128 + (lane & 31) * 16};
        v4u v[1];
        const int n = gather<1>(rs, off, tag, v, dead);
        STAMPN(18, n);
        float pA = __uint_as_float(v[0].x), pB = __uint_as_float(v[0].z);
        pA = tadd32(pA, pA); pB = tadd32(pB, pB);
        if (lane < 32) *(LAS u32*)(L + L_QS + (wave * 32 + lane) * 4) = pack_bf16(pA, pB);
        lds_barrier();
      }
      v4f sc = {0.f, 0.f, 0.f, 0.f};
#pragma unroll
      for (int s = 0; s < 4; ++s) {
        const v4u qf = *(const LAS v4u*)(L + L_QS + hq * 256 + s * 64 + gq * 16);
        sc = __builtin_amdgcn_mfma_f32_16x16x32_bf16(__builtin_bit_cast(v8bf, kf[s]), __builtin_bit_cast(v8bf, qf), sc, 0, 0, 0);
      }
      constexpr float C2 = 0.08838834764831845f * 1.4426950408889634f;
      float m = fmaxf(fmaxf(sc[0], sc[1]), fmaxf(sc[2], sc[3])) * C2;
      m = tmax16(m, m); m = tmax32(m, m);
      const float p0 = __builtin_amdgcn_exp2f(sc[0] * C2 - m), p1 = __builtin_amdgcn_exp2f(sc[1] * C2 - m);
      const float p2 = __builtin_amdgcn_exp2f(sc[2] * C2 - m), p3 = __builtin_amdgcn_exp2f(sc[3] * C2 - m);
      const u32 pa = pack_bf16(p0, p1), pb = pack_bf16(p2, p3);
      if (kr < 4) *(LAS v2u*)(L + L_P + wave * 128 + hq * 32 + gq * 8) = (v2u){pa, pb};
      float ls = (bf_lo(pa) + bf_hi(pa)) + (bf_lo(pb) + bf_hi(pb));
      ls = tadd16(ls, ls); ls = tadd32(ls, ls);
      if (lane < 4) *(LAS v2u*)(L + L_ML + hq * 64 + wave * 8) = (v2u){__float_as_uint(m), __float_as_uint(ls)};
      lds_barrier();
      v4f oacc[8];
#pragma unroll
      for (int t = 0; t < 8; ++t) {
        const v2u pf = *(const LAS v2u*)(L + L_P + t * 128 + hq * 32 + gq * 8);
        oacc[t] = __builtin_amdgcn_mfma_f32_16x16x16bf16_1k(__builtin_bit_cast(v4s, vf[t]), __builtin_bit_cast(v4s, pf), (v4f){0.f, 0.f, 0.f, 0.f}, 0, 0, 0);
      }
      float mw[8], lw[8];
#pragma unroll
      for (int t = 0; t < 4; ++t) {
        const v4u q4 = *(const LAS v4u*)(L + L_ML + hq * 64 + t * 16);
        mw[2 * t] = __uint_as_float(q4.x); lw[2 * t] = __uint_as_float(q4.y); mw[2 * t + 1] = __uint_as_float(q4.z); lw[2 * t + 1] = __uint_as_float(q4.w);
      }
      float M = mw[0];
#pragma unroll
      for (int t = 1; t < 8; ++t) M = fmaxf(M, mw[t]);
      float Lsum = 0.f; v4f o = {0.f, 0.f, 0.f, 0.f};
#pragma unroll
      for (int t = 0; t < 8; ++t) { const float e = __builtin_amdgcn_exp2f(mw[t] - M); Lsum = fmaf(e, lw[t], Lsum); o += oacc[t] * e; }
      const u32 pbase = xl + X_E2 + (u32)((g * 32 + j) * 17) * 128;
      if (kr < 4) {
        const v4u d = {pack_bf16(o[0], o[1]), tag, pack_bf16(o[2], o[3]), tag};
        __builtin_amdgcn_raw_buffer_store_b128(d, rs, pbase + (hq * 4 + (wave >> 1)) * 128 + ((wave & 1) * 4 + gq) * 16, 0, 16);
      }
      if (wave == 0 && lane < 4) {
        const v4u d = {__float_as_uint(M), tag, __float_as_uint(Lsum), tag};
        __builtin_amdgcn_raw_buffer_store_b128(d, rs, pbase + 16 * 128 + hq * 16, 0, 16);
      }
    }
    STAMP(5 + layer * 14);

    if (wave == 0 && (isC || layer != 0)) {
      __builtin_amdgcn_s_sleep(D_E2);
      u32 off[8];
#pragma unroll
      for (int i = 0; i < 4; ++i) {
        const u32 pb = xl + X_E2 + (u32)((g * 32 + 8 * i + (lane >> 3)) * 17) * 128;
        off[i] = pb + cidx * 128 + (lane & 7) * 16;
        off[4 + i] = pb + 16 * 128 + (cidx >> 2) * 16;
      }
      v4u v[8];
      const int n = gather<8>(rs, off, tag, v, dead);
      STAMPN(6 + layer * 14, n);
      float M = fmaxf(fmaxf(__uint_as_float(v[4].x), __uint_as_float(v[5].x)), fmaxf(__uint_as_float(v[6].x), __uint_as_float(v[7].x)));
      M = fmaxf(M, dppf<ROR8>(M)); M = tmax16(M, M); M = tmax32(M, M);
      float a0 = 0.f, a1 = 0.f, a2 = 0.f, a3 = 0.f, Ls = 0.f;
#pragma unroll
      for (int i = 0; i < 4; ++i) {
        const float e = __builtin_amdgcn_exp2f(__uint_as_float(v[4 + i].x) - M);
        Ls = fmaf(e, __uint_as_float(v[4 + i].z), Ls);
        a0 = fmaf(e, bf_lo(v[i].x), a0); a1 = fmaf(e, bf_hi(v[i].x), a1); a2 = fmaf(e, bf_lo(v[i].z), a2); a3 = fmaf(e, bf_hi(v[i].z), a3);
      }
      a0 += dppf<ROR8>(a0); a1 += dppf<ROR8>(a1); a2 += dppf<ROR8>(a2); a3 += dppf<ROR8>(a3); Ls += dppf<ROR8>(Ls);
      a0 = tadd16(a0, a0); a1 = tadd16(a1, a1); a2 = tadd16(a2, a2); a3 = tadd16(a3, a3); Ls = tadd16(Ls, Ls);
      a0 = tadd32(a0, a0); a1 = tadd32(a1, a1); a2 = tadd32(a2, a2); a3 = tadd32(a3, a3); Ls = tadd32(Ls, Ls);
      const float inv = __builtin_amdgcn_rcpf(Ls);
      if (isC && lane < 8) {
        const v4u d = {pack_bf16(a0 * inv, a1 * inv), tag, pack_bf16(a2 * inv, a3 * inv), tag};
        __builtin_amdgcn_raw_buffer_store_b128(d, rs, xl + X_E3 + (u32)(g * 16 + cidx) * 128 + lane * 16, 0, 16);
      }
      STAMP(7 + layer * 14);
    }
    if (layer == 0) {
      if (isD) { lds_barrier(); load_role_weights(); }
      else { asm volatile("s_waitcnt vmcnt(0)" ::: "memory"); lds_barrier(); }
    }

#pragma unroll
    for (int ph = 0; ph < 2; ++ph) {
      {
        if (ph == 1 && layer == 0 && isD) { asm volatile("s_waitcnt vmcnt(0)" ::: "memory"); lds_barrier(); }
        u32 wr[2][16];
        {
          v4u r[4][2];
#pragma unroll
          for (int i = 0; i < 4; ++i) {
            r[i][0] = *(const LAS v4u*)(L + L_WR + ((wave * 4 + i) * 64 + lane) * 32);
            r[i][1] = *(const LAS v4u*)(L + L_WR + ((wave * 4 + i) * 64 + lane) * 32 + 16);
          }
#pragma unroll
          for (int p = 0; p < 2; ++p)
#pragma unroll
            for (int hh = 0; hh < 2; ++hh)
#pragma unroll
              for (int c = 0; c < 4; ++c) {
                wr[p][hh * 8 + 2 * c] = perm_lo(r[2 * p][hh][c], r[2 * p + 1][hh][c]);
                wr[p][hh * 8 + 2 * c + 1] = perm_hi(r[2 * p][hh][c], r[2 * p + 1][hh][c]);
              }
        }
        lds_barrier();
        float part = 0.f;
        if (ph == 0 || wave < 7) {
          if (ph == 0) __builtin_amdgcn_s_sleep(D_E3); else __builtin_amdgcn_s_sleep(D_E5);
          const u32 off[1] = {xl + (ph == 0 ? X_E3 + (u32)okh * 64 * 128 : X_E5) + (u32)wave * 1024 + lane * 16};
          v4u v[1];
          const int n = gather<1>(rs, off, tag, v, dead);
          STAMPN(8 + ph * 4 + layer * 14, n);
          float ac[16];
#pragma unroll
          for (int c = 0; c < 16; ++c) ac[c] = dot2(v[0].z, wr[1][c], dot2(v[0].x, wr[0][c], 0.f));
          float t8[8], t4[4], t2[2];
#pragma unroll
          for (int c = 0; c < 8; ++c) t8[c] = tadd32(ac[c], ac[c + 8]);
#pragma unroll
          for (int c = 0; c < 4; ++c) t4[c] = tadd16(t8[c], t8[c + 4]);
#pragma unroll
          for (int c = 0; c < 2; ++c) { const float lo = t4[c] + dppf<ROR8>(t4[c]), hi = t4[c + 2] + dppf<ROR8>(t4[c + 2]); t2[c] = (lane & 8) ? hi : lo; }
          { const float lo = t2[0] + dppf<XOR7>(t2[0]), hi = t2[1] + dppf<XOR7>(t2[1]); part = (lane & 4) ? hi : lo; }
          part += dppf<XOR2>(part); part += dppf<XOR1>(part);
        }
        if ((lane & 3) == 0) *(LAS float*)(L + L_RED + (lane >> 2) * 32 + wave * 4) = part;
        lds_barrier();
        if (wave == 0) {
          const int c = lane & 15;
          const v4f r0 = *(const LAS v4f*)(L + L_RED + c * 32), r1 = *(const LAS v4f*)(L + L_RED + c * 32 + 16);
          const float sum = ((r0[0] + r0[1]) + (r0[2] + r0[3])) + ((r1[0] + r1[1]) + (r1[2] + r1[3]));
          if (ph == 0) {
            if (isO && lane < 16) {
              const v2u d = {__float_as_uint(sum), tag};
              __builtin_amdgcn_raw_buffer_store_b64(d, rs, xl + X_E4 + (u32)(okh * 32 + ocg) * 128 + lane * 8, 0, 16);
            }
          } else {
            const float bias = bf_lo(*(const LAS u16*)(L + L_B + 64 + c * 2));
            const float xp = bf_lo(*(const LAS u16*)(L + L_XP + (didx * 16 + c) * 2));
            const float r = rbf(xp + rbf(rbf(sum) + bias));
            const float rn = dppf<XOR1>(r);
            const u32 pr = pack_bf16(r, rn);
            if (isD && lane < 16 && !(lane & 1)) {
              if (layer == 0) {
                const v2u d = {pr, tag};
                __builtin_amdgcn_raw_buffer_store_b64(d, rs, xl + X_E6 + (u32)didx * 128 + (lane >> 1) * 8, 0, 16);
              } else {
                *(u32*)(a.out + didx * 16 + lane) = pr;
              }
            }
          }
          STAMP(9 + ph * 4 + layer * 14);
        }
      }
      if (ph == 0) {
        u32 wr[4][4];
        {
          v2u r[8];
#pragma unroll
          for (int i = 0; i < 8; ++i) r[i] = *(const LAS v2u*)(L + L_WR + (i * 64 + lane) * 64 + wave * 8);
#pragma unroll
          for (int p = 0; p < 4; ++p) {
            wr[p][0] = perm_lo(r[2 * p].x, r[2 * p + 1].x); wr[p][1] = perm_hi(r[2 * p].x, r[2 * p + 1].x);
            wr[p][2] = perm_lo(r[2 * p].y, r[2 * p + 1].y); wr[p][3] = perm_hi(r[2 * p].y, r[2 * p + 1].y);
          }
        }
        float cs;
        {
          float c0 = 0.f, c1 = 0.f, c2 = 0.f, c3 = 0.f;
#pragma unroll
          for (int p = 0; p < 4; ++p) { c0 = dot2(wr[p][0], ONES, c0); c1 = dot2(wr[p][1], ONES, c1); c2 = dot2(wr[p][2], ONES, c2); c3 = dot2(wr[p][3], ONES, c3); }
          cs = rowsum16(tadd16(tadd32(c0, c2), tadd32(c1, c3)));
        }
        const float bias = bf_lo(*(const LAS u16*)(L + L_B + (wave * 4 + (lane >> 4)) * 2));
        const u32 xres = *(const LAS u32*)(L + L_XS + (wave * 32 + (lane & 31)) * 4);
        lds_barrier();
        __builtin_amdgcn_s_sleep(D_E4);
        {
          const u32 off[1] = {xl + X_E4 + (u32)((lane >> 5) * 32 + wave * 4) * 128 + (lane & 31) * 16};
          v4u v[1];
          const int n = gather<1>(rs, off, tag, v, dead);
          STAMPN(10 + layer * 14, n);
          float pA = __uint_as_float(v[0].x), pB = __uint_as_float(v[0].z);
          pA = tadd32(pA, pA); pB = tadd32(pB, pB);
          const u32 at = pack_bf16(pA, pB);
          const u32 xp = pack_bf16(bf_lo(xres) + bf_lo(at), bf_hi(xres) + bf_hi(at));
          if (lane < 32) *(LAS u32*)(L + L_XP + (wave * 32 + lane) * 4) = xp;
        }
        lds_barrier();
        const v4u t = *(const LAS v4u*)(L + L_XP + lane * 16);
        const u32 xv[4] = {t.x, t.y, t.z, t.w};
        float a0 = 0.f, a1 = 0.f, a2 = 0.f, a3 = 0.f, s1 = 0.f, s2 = 0.f;
#pragma unroll
        for (int p = 0; p < 4; ++p) {
          a0 = dot2(xv[p], wr[p][0], a0); a1 = dot2(xv[p], wr[p][1], a1); a2 = dot2(xv[p], wr[p][2], a2); a3 = dot2(xv[p], wr[p][3], a3);
          s1 = dot2(xv[p], ONES, s1); s2 = dot2(xv[p], xv[p], s2);
        }
        const float acc = rowsum16(tadd16(tadd32(a0, a2), tadd32(a1, a3)));
        float ss = tadd32(s1, s2); ss = tadd16(ss, ss); ss = rowsum16(ss);
        const float mean = rdlane(ss, 0) * (1.f / 512.f);
        const float var = fmaxf(rdlane(ss, 32) * (1.f / 512.f) - mean * mean, 0.f);
        const float rstd = __builtin_amdgcn_rsqf(var + 1e-5f);
        const float z = rbf(rbf((acc - mean * cs) * rstd) + bias);
        const float u2 = z * (1.5957691216057308f + 0.07135481627260025f * z * z);
        const float y = z * __builtin_amdgcn_rcpf(1.f + __builtin_amdgcn_exp2f(-1.4426950408889634f * u2));
        const float y0 = rdlane(y, 0), y1 = rdlane(y, 16), y2 = rdlane(y, 32), y3 = rdlane(y, 48);
        if (isH && lane == 0) {
          const v4u d = {pack_bf16(y0, y1), tag, pack_bf16(y2, y3), tag};
          __builtin_amdgcn_raw_buffer_store_b128(d, rs, xl + X_E5 + (u32)hidx * 128 + wave * 16, 0, 16);
        }
        STAMP(11 + layer * 14);
      }
    }
    lds_barrier();
  }
  if (b == 0 && tid == 0) __hip_atomic_store((u32*)a.ws, tag, __ATOMIC_RELAXED, __HIP_MEMORY_SCOPE_AGENT);
  if (lane == 0) { *(LAS u64*)(L + L_ST + wave * 512 + 31 * 8) = __builtin_amdgcn_s_memtime(); *(LAS u64*)(L + L_ST + wave * 512 + 29 * 8) = __builtin_amdgcn_s_memrealtime(); }
  lds_barrier();
  if (b == 40 && wave == 0) {
    u64 mn = ~0ull, mx = 0;
    for (int i = 0; i < 4; ++i) { const int bb = lane * 4 + i; if (DIAG_X < 0 || (bb & 7) == DIAG_X) { const u64 t = __hip_atomic_load((u64*)(a.ws + xreg + 0x1c0000) + bb, __ATOMIC_RELAXED, __HIP_MEMORY_SCOPE_AGENT); mx = t > mx ? t : mx; }
      const u64 t2 = __hip_atomic_load((u64*)(a.ws + xreg + 0x1c0000) + bb, __ATOMIC_RELAXED, __HIP_MEMORY_SCOPE_AGENT); mn = t2 < mn ? t2 : mn; }
    for (int o = 32; o >= 1; o >>= 1) { u64 a1 = __shfl_xor(mn, o), a2 = __shfl_xor(mx, o); mn = a1 < mn ? a1 : mn; mx = a2 > mx ? a2 : mx; }
    const u64 own = *(LAS u64*)(L + L_ST);
    u64 d = (DIAG_OWN ? (own - mn) : (mx - mn)) * 10; if (d > 100000) d = 100000;
    const u64 t1 = __builtin_amdgcn_s_memrealtime() + d;
    while (__builtin_amdgcn_s_memrealtime() < t1) {}
  }
  ((u64*)(a.ws + X_DBG))[b * 512 + tid] = *(LAS u64*)(L + L_ST + tid * 8);
}

}

extern "C" void kernel_launch(void* const* d_in, const int* in_sizes, int n_in, void* d_out, int out_size, void* d_ws,
                              size_t ws_size, hipStream_t stream) {
  Args a;
  a.x = (const u16*)d_in[0]; a.kc = (const u16*)d_in[1]; a.vc = (const u16*)d_in[2]; a.wq = (const u16*)d_in[3];
  a.wo = (const u16*)d_in[4]; a.w1 = (const u16*)d_in[6]; a.b1 = (const u16*)d_in[7]; a.w2 = (const u16*)d_in[8];
  a.b2 = (const u16*)d_in[9]; a.out = (u16*)d_out; a.ws = (unsigned char*)d_ws;
  fused_decode<<<NWG, NTHR, 0, stream>>>(a);
}
```

```cpp
#include <hip/hip_runtime.h>
#include <stdint.h>

typedef unsigned u32;
typedef unsigned short u16;
typedef unsigned long long u64;
typedef u32 v4u __attribute__((ext_vector_type(4)));
typedef u32 v2u __attribute__((ext_vector_type(2)));
typedef float v4f __attribute__((ext_vector_type(4)));
typedef short v4s __attribute__((ext_vector_type(4)));
typedef __bf16 v8bf __attribute__((ext_vector_type(8)));
typedef __bf16 v2bf __attribute__((ext_vector_type(2)));
#define LAS __attribute__((address_space(3)))

#define STAMPS 0
#define D_E1 6
#define D_E2 8
#define D_E3 12
#define D_E4 10
#define D_E5 12
#define D_E6 10
#define D_KV0 0
#define D_WO 30
#define D_W12 60
#define D_BACK 6
#define D_Q0 80
#define D_G0 64
#define DMA_POL " nt"

namespace {

constexpr int SK = 65536, WIN = 4096;
constexpr int NWG = 256, NTHR = 512;

constexpr u32 L_K = 0, L_V = 65536, L_W4 = 0, L_WT = 32768, L_W1 = 65536;
constexpr u32 L_QS = 131072, L_XS = 132096, L_XP = 133120, L_P = 134144, L_ML = 136192, L_OS = 136448, L_HS = 136704,
              L_B = 136960, L_FLAG = 137984, L_ST = 138240, L_END = 138240 + 4096;

constexpr u32 X_E1 = 0;
constexpr u32 X_E2 = X_E1 + 128 * 128;
constexpr u32 X_E3 = X_E2 + 2176 * 128;
constexpr u32 X_E4 = X_E3 + 2048 * 128;
constexpr u32 X_E5 = X_E4 + 32 * 128;
constexpr u32 X_E6 = X_E5 + 1792 * 128;
constexpr u32 X_LAYER = X_E6 + 32 * 128;
constexpr u32 X_REGION = 2u << 20;
constexpr u32 X_NREG = 64;
constexpr u32 X_BASE = 2u << 20;
constexpr u32 X_DBG = X_BASE + X_NREG * X_REGION;
static_assert(2 * X_LAYER <= X_REGION, "region");

constexpr int SPIN_CAP = 1 << 18;

__device__ __forceinline__ void dma16(const void* gsrc, u32 lds_dst) {
  u32 keep;
  asm volatile("s_mov_b32 %0, m0\n\ts_mov_b32 m0, %2\n\ts_nop 0\n\tglobal_load_lds_dwordx4 %1, off" DMA_POL "\n\ts_mov_b32 m0, %0"
               : "=&s"(keep) : "v"(gsrc), "s"(__builtin_amdgcn_readfirstlane(lds_dst)) : "memory");
}
__device__ __forceinline__ void dma16_keep(const void* gsrc, u32 lds_dst) {
  u32 keep;
  asm volatile("s_mov_b32 %0, m0\n\ts_mov_b32 m0, %2\n\ts_nop 0\n\tglobal_load_lds_dwordx4 %1, off\n\ts_mov_b32 m0, %0"
               : "=&s"(keep) : "v"(gsrc), "s"(__builtin_amdgcn_readfirstlane(lds_dst)) : "memory");
}
__device__ __forceinline__ void lds_barrier() {
  asm volatile("s_waitcnt lgkmcnt(0)\n\ts_barrier" ::: "memory");
}
__device__ __forceinline__ float dot2(u32 a, u32 b, float c) {
  return __builtin_amdgcn_fdot2_f32_bf16(__builtin_bit_cast(v2bf, a), __builtin_bit_cast(v2bf, b), c, false);
}
__device__ __forceinline__ u32 pack_bf16(float lo, float hi) {
  u32 r;
  asm("v_cvt_pk_bf16_f32 %0, %1, %2" : "=v"(r) : "v"(lo), "v"(hi));
  return r;
}
__device__ __forceinline__ float bf_lo(u32 p) { return __uint_as_float(p << 16); }
__device__ __forceinline__ float bf_hi(u32 p) { return __uint_as_float(p & 0xffff0000u); }
__device__ __forceinline__ float rbf(float x) { return bf_lo(pack_bf16(x, x)); }
__device__ __forceinline__ u32 perm_lo(u32 a, u32 b) { return __builtin_amdgcn_perm(b, a, 0x05040100u); }
__device__ __forceinline__ u32 perm_hi(u32 a, u32 b) { return __builtin_amdgcn_perm(b, a, 0x07060302u); }
__device__ __forceinline__ float rdlane(float x, int l) { return __builtin_bit_cast(float, __builtin_amdgcn_readlane(__builtin_bit_cast(int, x), l)); }

template <int CTRL> __device__ __forceinline__ float dppf(float x) {
  return __builtin_bit_cast(float, __builtin_amdgcn_mov_dpp(__builtin_bit_cast(int, x), CTRL, 0xf, 0xf, true));
}
constexpr int XOR1 = 0xB1, XOR2 = 0x4E, XOR7 = 0x141, ROR8 = 0x128, ROR4 = 0x124, ROR2 = 0x122, ROR1 = 0x121;
constexpr u32 ONES = 0x3f803f80u;

__device__ __forceinline__ float tadd32(float a, float b) {
  auto s = __builtin_amdgcn_permlane32_swap(__float_as_uint(a), __float_as_uint(b), false, false);
  return __uint_as_float(s[0]) + __uint_as_float(s[1]);
}
__device__ __forceinline__ float tadd16(float a, float b) {
  auto s = __builtin_amdgcn_permlane16_swap(__float_as_uint(a), __float_as_uint(b), false, false);
  return __uint_as_float(s[0]) + __uint_as_float(s[1]);
}
__device__ __forceinline__ float vmax(float a, float b) { float r; asm("v_max_f32 %0, %1, %2" : "=v"(r) : "v"(a), "v"(b)); return r; }
__device__ __forceinline__ float vmax3(float a, float b, float c) { float r; asm("v_max3_f32 %0, %1, %2, %3" : "=v"(r) : "v"(a), "v"(b), "v"(c)); return r; }
__device__ __forceinline__ float tmax32(float a) {
  auto s = __builtin_amdgcn_permlane32_swap(__float_as_uint(a), __float_as_uint(a), false, false);
  return vmax(__uint_as_float(s[0]), __uint_as_float(s[1]));
}
__device__ __forceinline__ float tmax16(float a) {
  auto s = __builtin_amdgcn_permlane16_swap(__float_as_uint(a), __float_as_uint(a), false, false);
  return vmax(__uint_as_float(s[0]), __uint_as_float(s[1]));
}
__device__ __forceinline__ float rowsum16(float x) {
  x += dppf<ROR8>(x); x += dppf<ROR4>(x); x += dppf<ROR2>(x); x += dppf<ROR1>(x);
  return x;
}
__device__ __forceinline__ float reduce8(const float (&a)[8], int lane) {
  float t4[4], t2[2];
#pragma unroll
  for (int c = 0; c < 4; ++c) t4[c] = tadd32(a[c], a[c + 4]);
#pragma unroll
  for (int c = 0; c < 2; ++c) t2[c] = tadd16(t4[c], t4[c + 2]);
  const float lo = t2[0] + dppf<ROR8>(t2[0]), hi = t2[1] + dppf<ROR8>(t2[1]);
  float r = (lane & 8) ? hi : lo;
  r += dppf<XOR7>(r); r += dppf<XOR2>(r); r += dppf<XOR1>(r);
  return r;
}

template <int N>
__device__ __forceinline__ int gather(__amdgpu_buffer_rsrc_t rs, const u32 (&off)[N], u32 tag, v4u (&v)[N], bool& dead, bool slow = false) {
  int spin = 0;
  for (;; ++spin) {
    asm volatile("" ::: "memory");
#pragma unroll
    for (int i = 0; i < N; ++i) v[i] = __builtin_amdgcn_raw_buffer_load_b128(rs, off[i], 0, 16);
    bool ok = true;
#pragma unroll
    for (int i = 0; i < N; ++i) ok &= (v[i].y == tag) & (v[i].w == tag);
    if (__all(ok)) break;
    if (dead || spin > SPIN_CAP) { dead = true; break; }
    if (slow) __builtin_amdgcn_s_sleep(D_BACK);
  }
  return spin;
}

struct Args {
  const u16* x; const u16* kc; const u16* vc; const u16* wq; const u16* wo;
  const u16* w1; const u16* b1; const u16* w2; const u16* b2;
  u16* out; unsigned char* ws;
};

#define STAMP(i) do {} while (0)
#define STAMPN(i, n) do { (void)(n); } while (0)

#define TR16(t, ra, o0, o1, o2, o3, o4, o5, o6, o7, o8, o9, o10, o11, o12, o13, o14, o15)                                                                   \
  asm volatile("ds_read_b64_tr_b16 %0, %16 offset:" #o0 "\n\tds_read_b64_tr_b16 %1, %16 offset:" #o1 "\n\tds_read_b64_tr_b16 %2, %16 offset:" #o2                \
               "\n\tds_read_b64_tr_b16 %3, %16 offset:" #o3 "\n\tds_read_b64_tr_b16 %4, %16 offset:" #o4 "\n\tds_read_b64_tr_b16 %5, %16 offset:" #o5            \
               "\n\tds_read_b64_tr_b16 %6, %16 offset:" #o6 "\n\tds_read_b64_tr_b16 %7, %16 offset:" #o7 "\n\tds_read_b64_tr_b16 %8, %16 offset:" #o8            \
               "\n\tds_read_b64_tr_b16 %9, %16 offset:" #o9 "\n\tds_read_b64_tr_b16 %10, %16 offset:" #o10 "\n\tds_read_b64_tr_b16 %11, %16 offset:" #o11        \
               "\n\tds_read_b64_tr_b16 %12, %16 offset:" #o12 "\n\tds_read_b64_tr_b16 %13, %16 offset:" #o13 "\n\tds_read_b64_tr_b16 %14, %16 offset:" #o14      \
               "\n\tds_read_b64_tr_b16 %15, %16 offset:" #o15 "\n\ts_waitcnt lgkmcnt(0)"                                                                         \
               : "=&v"(t[0]), "=&v"(t[1]), "=&v"(t[2]), "=&v"(t[3]), "=&v"(t[4]), "=&v"(t[5]), "=&v"(t[6]), "=&v"(t[7]), "=&v"(t[8]), "=&v"(t[9]), "=&v"(t[10]), \
                 "=&v"(t[11]), "=&v"(t[12]), "=&v"(t[13]), "=&v"(t[14]), "=&v"(t[15])                                                                            \
               : "v"(ra) : "memory")

#define MFMA32(A, B, C) __builtin_amdgcn_mfma_f32_16x16x32_bf16(__builtin_bit_cast(v8bf, A), __builtin_bit_cast(v8bf, B), C, 0, 0, 0)

__device__ __forceinline__ float ln_gemv4(const v4u xv, const v4u (&w)[4], float cs) {
  float a0 = 0.f, a1 = 0.f, a2 = 0.f, a3 = 0.f, s1 = 0.f, s2 = 0.f;
#pragma unroll
  for (int p = 0; p < 4; ++p) {
    a0 = dot2(xv[p], w[p].x, a0); a1 = dot2(xv[p], w[p].y, a1); a2 = dot2(xv[p], w[p].z, a2); a3 = dot2(xv[p], w[p].w, a3);
    s1 = dot2(xv[p], ONES, s1); s2 = dot2(xv[p], xv[p], s2);
  }
  const float acc = rowsum16(tadd16(tadd32(a0, a2), tadd32(a1, a3)));
  float ss = tadd32(s1, s2); ss = tadd16(ss, ss); ss = rowsum16(ss);
  const float mean = rdlane(ss, 0) * (1.f / 512.f);
  const float var = fmaxf(rdlane(ss, 32) * (1.f / 512.f) - mean * mean, 0.f);
  const float rstd = __builtin_amdgcn_rsqf(var + 1e-5f);
  return (acc - mean * cs) * rstd;
}

__global__ void __launch_bounds__(NTHR, 2) fused_decode(Args a) {
  __shared__ __attribute__((aligned(16))) unsigned char smem[L_END];
  LAS unsigned char* L = (LAS unsigned char*)smem;
  const u32 lbase = (u32)(size_t)L;
  const int tid = threadIdx.x, lane = tid & 63, wave = __builtin_amdgcn_readfirstlane(tid >> 6);
  const int b = blockIdx.x, g = b & 7, j = b >> 3;

  const bool isA = j < 16, isR = j < 4, isC = (j >= 16) & (j < 24), isH = (j >= 24) & (j < 31);
  const int jj = j & 15;
  const int ci = j & 7;
  const int hidx = g * 7 + (isH ? j - 24 : 0);
  const int ri = 4 * g + (j & 3);

  u32 epoch;
  if (isA) {
    if (tid == 0) *(volatile LAS u32*)(L + L_FLAG) = 0u;
    asm volatile("s_waitcnt lgkmcnt(0)\n\ts_barrier" ::: "memory");
  }
  asm volatile("s_load_dword %0, %1, 0x0" : "=s"(epoch) : "s"(a.ws) : "memory");
  if (isA) {
    if (wave >= 4) {
      const int w4 = wave - 4;
      __builtin_amdgcn_s_sleep(D_KV0);
      const size_t kvoff = ((size_t)g * SK + (SK - WIN) + jj * 256) * 256;
      const int r0 = w4 * 4 + (lane >> 4);
      const char* kp = (const char*)a.kc + kvoff + r0 * 256 + ((lane & 15) ^ (r0 & 15)) * 16;
#pragma unroll
      for (int t = 0; t < 16; ++t) dma16(kp + t * 4096, lbase + L_K + (t * 4 + w4) * 1024);
      const char* vp = (const char*)a.vc + kvoff + r0 * 256 + ((lane & 15) ^ ((r0 & 7) << 1)) * 16;
#pragma unroll
      for (int t = 0; t < 16; ++t) dma16(vp + t * 4096, lbase + L_V + (t * 4 + w4) * 1024);
    }
  } else {
#pragma unroll
    for (int t = 0; t < 4; ++t) {
      const int tt = (t + 2 * (j & 1)) & 3, lp = tt * 16 + (lane >> 2);
      dma16((const char*)a.wq + (size_t)(8 * lp + wave) * 8192 + g * 1024 + jj * 64 + ((lane & 3) ^ ((lp >> 2) & 3)) * 16, lbase + L_W4 + (wave * 4 + tt) * 1024);
    }
  }
  if (wave == 0) dma16((const char*)a.x + lane * 16, lbase + L_XS);
  if (wave == 1) {
    const char* src = (lane & 7) < 4 ? (const char*)a.b1 + hidx * 64 + (lane & 3) * 16 : (const char*)a.b2 + ri * 32 + (lane & 1) * 16;
    dma16(src, lbase + L_B);
  }
  asm volatile("s_waitcnt lgkmcnt(0)" : "+s"(epoch) :: "memory");
  const u32 tag = epoch + 1u;
  const u32 xreg = X_BASE + (epoch % X_NREG) * X_REGION;
  const __amdgpu_buffer_rsrc_t rs = __builtin_amdgcn_make_buffer_rsrc(a.ws + xreg, 0, X_REGION, 0x00020000);

  bool dead = false;
  const int gq = lane >> 4, hq = lane & 3, kr = lane & 15;
  v4u RA[8], RB[8], RH[4];
  float r_cs = 0.f, h_cs = 0.f, h_bias = 0.f;
  u32 xkeep = 0u;
#pragma unroll
  for (int i = 0; i < 8; ++i) { RA[i] = (v4u){0u, 0u, 0u, 0u}; RB[i] = (v4u){0u, 0u, 0u, 0u}; }
#pragma unroll
  for (int i = 0; i < 4; ++i) RH[i] = (v4u){0u, 0u, 0u, 0u};

  auto prep4 = [&](u32 base, v4u& w0, v4u& w1, v4u& w2, v4u& w3) -> float {
    int ln = tid & 63; asm volatile("" : "+v"(ln));
    v2u r[8];
#pragma unroll
    for (int i = 0; i < 8; ++i) r[i] = *(const LAS v2u*)(L + base + (i * 64 + ln) * 64 + ((wave >> 1) ^ ((ln >> 2) & 3)) * 16 + (wave & 1) * 8);
    v4u w[4];
#pragma unroll
    for (int p = 0; p < 4; ++p)
      w[p] = (v4u){perm_lo(r[2 * p].x, r[2 * p + 1].x), perm_hi(r[2 * p].x, r[2 * p + 1].x), perm_lo(r[2 * p].y, r[2 * p + 1].y), perm_hi(r[2 * p].y, r[2 * p + 1].y)};
    float c0 = 0.f, c1 = 0.f, c2 = 0.f, c3 = 0.f;
#pragma unroll
    for (int p = 0; p < 4; ++p) { c0 = dot2(w[p].x, ONES, c0); c1 = dot2(w[p].y, ONES, c1); c2 = dot2(w[p].z, ONES, c2); c3 = dot2(w[p].w, ONES, c3); }
    w0 = w[0]; w1 = w[1]; w2 = w[2]; w3 = w[3];
    return rowsum16(tadd16(tadd32(c0, c2), tadd32(c1, c3)));
  };
  auto tr_addr = [&](int c, int half) -> u32 {
    int ln = tid & 63; asm volatile("" : "+v"(ln));
    const int row = 8 * (ln >> 4) + 4 * half + ((ln & 15) >> 2), p = ln & 3;
    return lbase + L_WT + row * 1024 + (((8 * wave + 2 * c + (p >> 1)) ^ ((row & 7) << 1)) * 16) + (p & 1) * 8;
  };
  auto store_partial = [&](v4f c0, v4f c1, v4f c2, v4f c3, u32 base, bool act) {
    float v[4];
#pragma unroll
    for (int r = 0; r < 4; ++r) {
      const float lo = (lane & 1) ? c1[r] : c0[r], hi = (lane & 1) ? c3[r] : c2[r];
      v[r] = (lane & 2) ? hi : lo;
    }
    if (act && kr < 4) {
      const u32 o = base + (u32)(wave * 4 + hq) * 128 + gq * 32;
      __builtin_amdgcn_raw_buffer_store_b128((v4u){__float_as_uint(v[0]), tag, __float_as_uint(v[1]), tag}, rs, o, 0, 16);
      __builtin_amdgcn_raw_buffer_store_b128((v4u){__float_as_uint(v[2]), tag, __float_as_uint(v[3]), tag}, rs, o + 16, 0, 16);
    }
  };
  STAMP(1);

  for (int layer = 0; layer < 2; ++layer) {
    const u32 xl = layer * X_LAYER;
    if (layer != 0 || !isA) {
      if (layer == 0) {
        asm volatile("s_waitcnt vmcnt(0)" ::: "memory");
        lds_barrier();
        r_cs = prep4(L_W4, RH[0], RH[1], RH[2], RH[3]);
        STAMP(2);
      } else {
        if (wave == 0) {
          __builtin_amdgcn_s_sleep(D_E6);
          const u32 o0 = X_E6 + (lane >> 1) * 128 + (lane & 1) * 32;
          const u32 off[2] = {o0, o0 + 16};
          v4u v[2];
          const int n = gather<2>(rs, off, tag, v, dead);
          STAMPN(16, n);
          *(LAS v4u*)(L + L_XS + lane * 16) = (v4u){v[0].x, v[0].z, v[1].x, v[1].z};
        }
        lds_barrier();
      }
      const v4u xv = *(const LAS v4u*)(L + L_XS + lane * 16);
      const float y = ln_gemv4(xv, RH, r_cs);
      const float y0 = rdlane(y, 0), y1 = rdlane(y, 16), y2 = rdlane(y, 32), y3 = rdlane(y, 48);
      if (!isA && lane == 0) {
        const v4u d = {pack_bf16(y0, y1), tag, pack_bf16(y2, y3), tag};
        __builtin_amdgcn_raw_buffer_store_b128(d, rs, xl + X_E1 + (u32)(g * 16 + jj) * 128 + wave * 16, 0, 16);
      }
      STAMP(3 + layer * 14);
    }
    if (layer == 0) {
      const v4u z = {0u, ~tag, 0u, ~tag};
      int t_ = tid; asm volatile("" : "+v"(t_));
      if (isA) {
        if (wave >= 1 && wave < 4) {
#pragma unroll
          for (int rep = 0; rep < 2; ++rep) {
            const int pc = t_ - 64 + rep * 192, lay = pc >= 136;
            if (pc < 272) __builtin_amdgcn_raw_buffer_store_b128(z, rs, X_E2 + (u32)((g * 16 + jj) * 17) * 128 + lay * X_LAYER + (pc - lay * 136) * 16, 0, 16);
            else if (isR && pc < 296) { const int line = (pc - 272) >> 3;
              __builtin_amdgcn_raw_buffer_store_b128(z, rs, (line < 2 ? line * X_LAYER + X_E4 : X_E6) + (u32)ri * 128 + (pc & 7) * 16, 0, 16); }
          }
        }
      } else {
        if (isC | isH) __builtin_amdgcn_raw_buffer_store_b128(z, rs, (isC ? X_E3 + (u32)(g * 8 + ci) * 4096 : X_E5 + (u32)hidx * 4096) + (t_ >> 8) * X_LAYER + (t_ & 255) * 16, 0, 16);
        if (t_ < 8) __builtin_amdgcn_raw_buffer_store_b128(z, rs, X_LAYER + X_E1 + (u32)(g * 16 + jj) * 128 + t_ * 16, 0, 16);
        if (isC | isH) {
          lds_barrier();
          int ln = tid & 63; asm volatile("" : "+v"(ln));
          if (isC) {
            __builtin_amdgcn_s_sleep(D_WO);
            const char* p = (const char*)a.wo + (size_t)(g * 512 + ci * 64 + wave) * 1024 + (ln ^ (wave << 1)) * 16;
#pragma unroll
            for (int t = 0; t < 8; ++t) dma16(p + t * 8192, lbase + L_WT + (t * 8 + wave) * 1024);
          } else {
            __builtin_amdgcn_s_sleep(D_W12);
#pragma unroll
            for (int t = 0; t < 4; ++t) {
              const int tt = (t + 2 * (hidx & 1)) & 3, lp = tt * 16 + (ln >> 2);
              dma16((const char*)a.w1 + (size_t)(8 * lp + wave) * 3584 + hidx * 64 + ((ln & 3) ^ ((lp >> 2) & 3)) * 16, lbase + L_W1 + (wave * 4 + tt) * 1024);
            }
            const char* p = (const char*)a.w2 + (size_t)(hidx * 32 + wave) * 1024 + (ln ^ (wave << 1)) * 16;
#pragma unroll
            for (int t = 0; t < 4; ++t) dma16(p + t * 8192, lbase + L_WT + (t * 8 + wave) * 1024);
          }
          asm volatile("s_waitcnt vmcnt(0)" ::: "memory");
          STAMP(15);
          lds_barrier();
          {
            v2u t[16];
            const u32 a0 = tr_addr(0, 0), a1 = tr_addr(0, 1), a2 = tr_addr(1, 0), a3 = tr_addr(1, 1), a4 = tr_addr(2, 0), a5 = tr_addr(2, 1), a6 = tr_addr(3, 0), a7 = tr_addr(3, 1);
            asm volatile(
                "ds_read_b64_tr_b16 %0, %16\n\tds_read_b64_tr_b16 %1, %17\n\tds_read_b64_tr_b16 %2, %16 offset:32768\n\tds_read_b64_tr_b16 %3, %17 offset:32768\n\t"
                "ds_read_b64_tr_b16 %4, %18\n\tds_read_b64_tr_b16 %5, %19\n\tds_read_b64_tr_b16 %6, %18 offset:32768\n\tds_read_b64_tr_b16 %7, %19 offset:32768\n\t"
                "ds_read_b64_tr_b16 %8, %20\n\tds_read_b64_tr_b16 %9, %21\n\tds_read_b64_tr_b16 %10, %20 offset:32768\n\tds_read_b64_tr_b16 %11, %21 offset:32768\n\t"
                "ds_read_b64_tr_b16 %12, %22\n\tds_read_b64_tr_b16 %13, %23\n\tds_read_b64_tr_b16 %14, %22 offset:32768\n\tds_read_b64_tr_b16 %15, %23 offset:32768\n\t"
                "s_waitcnt lgkmcnt(0)"
                : "=&v"(t[0]), "=&v"(t[1]), "=&v"(t[2]), "=&v"(t[3]), "=&v"(t[4]), "=&v"(t[5]), "=&v"(t[6]), "=&v"(t[7]), "=&v"(t[8]), "=&v"(t[9]), "=&v"(t[10]),
                  "=&v"(t[11]), "=&v"(t[12]), "=&v"(t[13]), "=&v"(t[14]), "=&v"(t[15])
                : "v"(a0), "v"(a1), "v"(a2), "v"(a3), "v"(a4), "v"(a5), "v"(a6), "v"(a7) : "memory");
#pragma unroll
            for (int c = 0; c < 4; ++c) {
              RB[2 * c] = (v4u){t[4 * c].x, t[4 * c].y, t[4 * c + 1].x, t[4 * c + 1].y};
              RB[2 * c + 1] = (v4u){t[4 * c + 2].x, t[4 * c + 2].y, t[4 * c + 3].x, t[4 * c + 3].y};
            }
          }
          if (isH) {
            h_cs = prep4(L_W1, RA[0], RA[1], RA[2], RA[3]);
            h_bias = bf_lo(*(const LAS u16*)(L + L_B + (wave * 4 + (ln >> 4)) * 2));
          }
          STAMP(28);
        }
      }
    }

    if (layer != 0 || isA) {
      if (layer != 0) lds_barrier();
      if (wave == 0) {
        if (layer != 0) __builtin_amdgcn_s_sleep(D_E1); else __builtin_amdgcn_s_sleep(D_Q0);
        const u32 o0 = xl + X_E1 + (u32)(g * 16 + (lane >> 2)) * 128 + (lane & 3) * 32;
        const u32 off[2] = {o0, o0 + 16};
        v4u v[2];
        if (layer == 0) {
          v[0] = __builtin_amdgcn_raw_buffer_load_b128(rs, off[0], 0, 16); v[1] = __builtin_amdgcn_raw_buffer_load_b128(rs, off[1], 0, 16);
          asm volatile("" ::: "memory");
          if (lane == 0) *(volatile LAS u32*)(L + L_FLAG) = 1u;
          asm volatile("" ::: "memory");
        }
        int n = 0;
        if (layer != 0 || !__all((v[0].y == tag) & (v[0].w == tag) & (v[1].y == tag) & (v[1].w == tag))) n = 1 + gather<2>(rs, off, tag, v, dead, layer == 0);
        STAMPN(4 + layer * 14, n - (layer != 0));
        *(LAS v4u*)(L + L_QS + ((lane >> 2) & 3) * 256 + (lane >> 4) * 64 + (lane & 3) * 16) = (v4u){v[0].x, v[0].z, v[1].x, v[1].z};
      } else if (layer == 0 && wave >= 4) {
        asm volatile("s_waitcnt vmcnt(16)" ::: "memory");
        STAMP(4);
      }
      lds_barrier();
      if (layer == 0) {
#pragma unroll
        for (int s = 0; s < 8; ++s) RA[s] = *(const LAS v4u*)(L + L_K + (wave * 32 + (s >> 2) * 16 + kr) * 256 + (((4 * (s & 3) + gq) ^ kr) * 16));
      }
      v4f sa = {0.f, 0.f, 0.f, 0.f}, sb = {0.f, 0.f, 0.f, 0.f};
      {
        v4u qf[4];
#pragma unroll
        for (int s = 0; s < 4; ++s) qf[s] = *(const LAS v4u*)(L + L_QS + s * 256 + hq * 64 + gq * 16);
#pragma unroll
        for (int s = 0; s < 4; ++s) { sa = MFMA32(RA[s], qf[s], sa); sb = MFMA32(RA[4 + s], qf[s], sb); }
      }
      constexpr float C2 = 0.08838834764831845f * 1.4426950408889634f;
      sa *= C2; sb *= C2;
      float m = vmax3(vmax3(sa[0], sa[1], sa[2]), vmax3(sa[3], sb[0], sb[1]), vmax(sb[2], sb[3]));
      m = tmax16(m); m = tmax32(m);
      if (lane < 4) *(LAS float*)(L + L_ML + hq * 32 + wave * 4) = m;
      lds_barrier();
      const v4f m0 = *(const LAS v4f*)(L + L_ML + hq * 32), m1 = *(const LAS v4f*)(L + L_ML + hq * 32 + 16);
      const float M2 = vmax(vmax3(vmax3(m0[0], m0[1], m0[2]), m0[3], m1[0]), vmax3(m1[1], m1[2], m1[3]));
      if (kr < 4) {
        float p[8];
#pragma unroll
        for (int r = 0; r < 4; ++r) { p[r] = __builtin_amdgcn_exp2f(sa[r] - M2); p[4 + r] = __builtin_amdgcn_exp2f(sb[r] - M2); }
        *(LAS v4u*)(L + L_P + wave * 256 + hq * 64 + gq * 16) = (v4u){pack_bf16(p[0], p[1]), pack_bf16(p[2], p[3]), pack_bf16(p[4], p[5]), pack_bf16(p[6], p[7])};
      }
      if (layer == 0) {
        if (wave >= 4) asm volatile("s_waitcnt vmcnt(0)" ::: "memory");
        STAMP(14);
      }
      lds_barrier();
      if (layer == 0) {
        const int row0 = 4 * gq + ((lane & 15) >> 2), vp = lane & 3;
        const u32 va = lbase + L_V + row0 * 256 + (((2 * wave + (vp >> 1)) ^ ((row0 & 7) << 1)) * 16) + (vp & 1) * 8;
        v2u t[16];
        TR16(t, va, 0, 4096, 8192, 12288, 16384, 20480, 24576, 28672, 32768, 36864, 40960, 45056, 49152, 53248, 57344, 61440);
#pragma unroll
        for (int u = 0; u < 8; ++u) RB[u] = (v4u){t[2 * u].x, t[2 * u].y, t[2 * u + 1].x, t[2 * u + 1].y};
      }
      v4u pf[8];
#pragma unroll
      for (int u = 0; u < 8; ++u) pf[u] = *(const LAS v4u*)(L + L_P + u * 256 + hq * 64 + gq * 16);
      v4f oa = {0.f, 0.f, 0.f, 0.f}, ob = {0.f, 0.f, 0.f, 0.f};
#pragma unroll
      for (int u = 0; u < 4; ++u) { oa = MFMA32(RB[2 * u], pf[2 * u], oa); ob = MFMA32(RB[2 * u + 1], pf[2 * u + 1], ob); }
      const v4f o = oa + ob;
      const u32 pbase = xl + X_E2 + (u32)((g * 16 + jj) * 17) * 128;
      if (isA && kr < 4) {
        const v4u d = {pack_bf16(o[0], o[1]), tag, pack_bf16(o[2], o[3]), tag};
        __builtin_amdgcn_raw_buffer_store_b128(d, rs, pbase + (hq * 4 + (wave >> 1)) * 128 + ((wave & 1) * 4 + gq) * 16, 0, 16);
      }
      if (wave == 7) {
        float l0 = 0.f, l1 = 0.f;
#pragma unroll
        for (int u = 0; u < 8; ++u) { l0 = dot2(pf[u].x, ONES, l0); l1 = dot2(pf[u].y, ONES, l1); l0 = dot2(pf[u].z, ONES, l0); l1 = dot2(pf[u].w, ONES, l1); }
        float ls = l0 + l1;
        ls = tadd16(ls, ls); ls = tadd32(ls, ls);
        if (isA && lane < 4) {
          const v4u d = {__float_as_uint(M2), tag, __float_as_uint(ls), tag};
          __builtin_amdgcn_raw_buffer_store_b128(d, rs, pbase + 16 * 128 + hq * 16, 0, 16);
        }
      }
      STAMP(5 + layer * 14);
    }

    {
      if (wave == 0) {
        __builtin_amdgcn_s_sleep(D_E2);
        if (layer == 0 && !isA && !isC && !isH) __builtin_amdgcn_s_sleep(D_G0);
        u32 off[8];
#pragma unroll
        for (int i = 0; i < 4; ++i) {
          const u32 pb = xl + X_E2 + (u32)((g * 16 + 4 * i + gq) * 17) * 128;
          off[i] = pb + ci * 256 + kr * 16;
          off[4 + i] = pb + 16 * 128 + (ci >> 1) * 16;
        }
        v4u v[8];
        const int n = gather<8>(rs, off, tag, v, dead, layer == 0);
        STAMPN(6 + layer * 14, n);
        float M = vmax(vmax3(__uint_as_float(v[4].x), __uint_as_float(v[5].x), __uint_as_float(v[6].x)), __uint_as_float(v[7].x));
        M = tmax16(M); M = tmax32(M);
        float a0 = 0.f, a1 = 0.f, a2 = 0.f, a3 = 0.f, Ls = 0.f;
#pragma unroll
        for (int i = 0; i < 4; ++i) {
          const float e = __builtin_amdgcn_exp2f(__uint_as_float(v[4 + i].x) - M);
          Ls = fmaf(e, __uint_as_float(v[4 + i].z), Ls);
          a0 = fmaf(e, bf_lo(v[i].x), a0); a1 = fmaf(e, bf_hi(v[i].x), a1); a2 = fmaf(e, bf_lo(v[i].z), a2); a3 = fmaf(e, bf_hi(v[i].z), a3);
        }
        float b0 = tadd32(a0, a2), b1 = tadd32(a1, a3); Ls = tadd32(Ls, Ls);
        b0 = tadd16(b0, b0); b1 = tadd16(b1, b1); Ls = tadd16(Ls, Ls);
        const float inv = __builtin_amdgcn_rcpf(Ls);
        if (!(lane & 16)) *(LAS u32*)(L + L_OS + kr * 8 + (lane >> 5) * 4) = pack_bf16(b0 * inv, b1 * inv);
      }
      lds_barrier();
      const v4u of0 = *(const LAS v4u*)(L + L_OS + gq * 16), of1 = *(const LAS v4u*)(L + L_OS + 64 + gq * 16);
      v4f acc[4];
#pragma unroll
      for (int c = 0; c < 4; ++c) acc[c] = MFMA32(RB[2 * c + 1], of1, MFMA32(RB[2 * c], of0, ((v4f){0.f, 0.f, 0.f, 0.f})));
      store_partial(acc[0], acc[1], acc[2], acc[3], xl + X_E3 + (u32)(g * 8 + ci) * 4096, isC);
      STAMP(7 + layer * 14);
    }

#pragma unroll
    for (int ph = 0; ph < 2; ++ph) {
      lds_barrier();
      if (wave == 0) {
        constexpr int NP = 8;
        if (ph == 0) __builtin_amdgcn_s_sleep(D_E3); else __builtin_amdgcn_s_sleep(D_E5);
        u32 off[NP];
#pragma unroll
        for (int i = 0; i < NP; ++i) off[i] = xl + (ph == 0 ? X_E3 : X_E5) + (u32)(((ph == 1 && i == 7 ? 6 : i) * 8 + (lane >> 3)) * 32 + ri) * 128 + (lane & 7) * 16;
        const u32 xin = ph == 0 ? *(const LAS u32*)(L + L_XS + ri * 32 + (lane & 7) * 4) : xkeep;
        const u32 bb = *(const LAS u32*)(L + L_B + 64 + (lane & 7) * 4);
        v4u v[NP];
        const int n = gather<NP>(rs, off, tag, v, dead);
        STAMPN(8 + ph * 4 + layer * 14, n);
        float s0 = 0.f, s1 = 0.f;
#pragma unroll
        for (int i = 0; i < (ph == 0 ? 8 : 7); ++i) { s0 += __uint_as_float(v[i].x); s1 += __uint_as_float(v[i].z); }
        float t = tadd32(s0, s1); t = tadd16(t, t); t += dppf<ROR8>(t);
        float r = rbf(t);
        if (ph == 1) r = rbf(r + ((lane & 32) ? bf_hi(bb) : bf_lo(bb)));
        r = r + ((lane & 32) ? bf_hi(xin) : bf_lo(xin));
        auto sw = __builtin_amdgcn_permlane32_swap(__float_as_uint(r), __float_as_uint(r), false, false);
        const u32 pr = pack_bf16(__uint_as_float(sw[0]), __uint_as_float(sw[1]));
        xkeep = pr;
        if (isR && lane < 8) {
          if (ph == 0 || layer == 0) {
            const v2u d = {pr, tag};
            __builtin_amdgcn_raw_buffer_store_b64(d, rs, xl + (ph == 0 ? X_E4 : X_E6) + (u32)ri * 128 + lane * 8, 0, 16);
          } else {
            *(u32*)(a.out + ri * 16 + lane * 2) = pr;
          }
        }
        STAMP(9 + ph * 4 + layer * 14);
      }
      if (ph == 0) {
        if (wave == 0) {
          __builtin_amdgcn_s_sleep(D_E4);
          const u32 o0 = xl + X_E4 + (lane >> 1) * 128 + (lane & 1) * 32;
          const u32 off[2] = {o0, o0 + 16};
          v4u v[2];
          const int n = gather<2>(rs, off, tag, v, dead);
          STAMPN(10 + layer * 14, n);
          *(LAS v4u*)(L + L_XP + lane * 16) = (v4u){v[0].x, v[0].z, v[1].x, v[1].z};
        }
        lds_barrier();
        const v4u xv = *(const LAS v4u*)(L + L_XP + lane * 16);
        const v4u w1r[4] = {RA[0], RA[1], RA[2], RA[3]};
        const float z = rbf(rbf(ln_gemv4(xv, w1r, h_cs)) + h_bias);
        const float u2 = z * (1.5957691216057308f + 0.07135481627260025f * z * z);
        const float y = z * __builtin_amdgcn_rcpf(1.f + __builtin_amdgcn_exp2f(-1.4426950408889634f * u2));
        if (kr == 0) *(LAS u16*)(L + L_HS + (wave * 4 + gq) * 2) = (u16)pack_bf16(y, y);
        lds_barrier();
        const v4u hf = *(const LAS v4u*)(L + L_HS + gq * 16);
        v4f acc[4];
#pragma unroll
        for (int c = 0; c < 4; ++c) acc[c] = MFMA32(RB[2 * c], hf, ((v4f){0.f, 0.f, 0.f, 0.f}));
        store_partial(acc[0], acc[1], acc[2], acc[3], xl + X_E5 + (u32)hidx * 4096, isH);
        STAMP(11 + layer * 14);
      }
    }
  }
  if (b == 0 && tid == 0) __hip_atomic_store((u32*)a.ws, tag, __ATOMIC_RELAXED, __HIP_MEMORY_SCOPE_AGENT);
}

}

extern "C" void kernel_launch(void* const* d_in, const int* in_sizes, int n_in, void* d_out, int out_size, void* d_ws,
                              size_t ws_size, hipStream_t stream) {
  Args a;
  a.x = (const u16*)d_in[0]; a.kc = (const u16*)d_in[1]; a.vc = (const u16*)d_in[2]; a.wq = (const u16*)d_in[3];
  a.wo = (const u16*)d_in[4]; a.w1 = (const u16*)d_in[6]; a.b1 = (const u16*)d_in[7]; a.w2 = (const u16*)d_in[8];
  a.b2 = (const u16*)d_in[9]; a.out = (u16*)d_out; a.ws = (unsigned char*)d_ws;
  fused_decode<<<NWG, NTHR, 0, stream>>>(a);
}
```

```cpp
#include <hip/hip_runtime.h>
#include <stdint.h>

typedef unsigned u32;
typedef unsigned short u16;
typedef unsigned long long u64;
typedef u32 v4u __attribute__((ext_vector_type(4)));
typedef u32 v2u __attribute__((ext_vector_type(2)));
typedef float v4f __attribute__((ext_vector_type(4)));
typedef short v4s __attribute__((ext_vector_type(4)));
typedef __bf16 v8bf __attribute__((ext_vector_type(8)));
typedef __bf16 v2bf __attribute__((ext_vector_type(2)));
#define LAS __attribute__((address_space(3)))

#define STAMPS 0
#define D_E1 6
#define D_E2 7
#define D_E3 15
#define D_E4 12
#define D_E5 14
#define D_E6 12
#define D_KV0 8
#define D_WO 20
#define D_W12 1
#define D_V0 8
#define KV_ROT 1
#define KV_WIN 14
#define KV_SPLIT 24
#define PREW 3
#define SAFE_RANGE 60.f
#define D_BACK 2
#define D_Q0 60
#define D_G0 64
#define DMA_POL " nt"

namespace {

constexpr int SK = 65536, WIN = 4096;
constexpr int NWG = 256, NTHR = 512;

constexpr u32 L_K = 0, L_V = 65536, L_W4 = 0, L_WT = 32768, L_W1 = 65536;
constexpr u32 L_QS = 131072, L_XS = 132096, L_XP = 133120, L_P = 134144, L_ML = 136192, L_OS = 136448, L_HS = 136704,
              L_B = 136960, L_FLAG = 137984, L_ST = 138240, L_SCR = 138240 + 4096, L_OW = 138240 + 4096 + 3072, L_END = 138240 + 4096 + 3072 + 16384;

constexpr u32 X_E1 = 0;
constexpr u32 X_E2 = X_E1 + 512 * 128;
constexpr u32 X_E3 = X_E2 + 2176 * 128;
constexpr u32 X_E4 = X_E3 + 2048 * 128;
constexpr u32 X_E5 = X_E4 + 32 * 128;
constexpr u32 X_E6 = X_E5 + 1792 * 128;
constexpr u32 X_LAYER = X_E6 + 32 * 128;
constexpr u32 X_REGION = 2u << 20;
constexpr u32 X_NREG = 64;
constexpr u32 X_BASE = 2u << 20;
constexpr u32 X_DBG = X_BASE + X_NREG * X_REGION;
static_assert(2 * X_LAYER <= X_REGION, "region");

#define CODE_BYTES 14336u
constexpr int SPIN_CAP = 1 << 18;

__device__ __forceinline__ void dma16(const void* gsrc, u32 lds_dst) {
  u32 keep;
  asm volatile("s_mov_b32 %0, m0\n\ts_mov_b32 m0, %2\n\ts_nop 0\n\tglobal_load_lds_dwordx4 %1, off" DMA_POL "\n\ts_mov_b32 m0, %0"
               : "=&s"(keep) : "v"(gsrc), "s"(__builtin_amdgcn_readfirstlane(lds_dst)) : "memory");
}
__device__ __forceinline__ void dma16_keep(const void* gsrc, u32 lds_dst) {
  u32 keep;
  asm volatile("s_mov_b32 %0, m0\n\ts_mov_b32 m0, %2\n\ts_nop 0\n\tglobal_load_lds_dwordx4 %1, off\n\ts_mov_b32 m0, %0"
               : "=&s"(keep) : "v"(gsrc), "s"(__builtin_amdgcn_readfirstlane(lds_dst)) : "memory");
}
__device__ __forceinline__ void lds_barrier() {
  asm volatile("s_waitcnt lgkmcnt(0)\n\ts_barrier" ::: "memory");
}
__device__ __forceinline__ float dot2(u32 a, u32 b, float c) {
  return __builtin_amdgcn_fdot2_f32_bf16(__builtin_bit_cast(v2bf, a), __builtin_bit_cast(v2bf, b), c, false);
}
__device__ __forceinline__ u32 pack_bf16(float lo, float hi) {
  u32 r;
  asm("v_cvt_pk_bf16_f32 %0, %1, %2" : "=v"(r) : "v"(lo), "v"(hi));
  return r;
}
__device__ __forceinline__ float bf_lo(u32 p) { return __uint_as_float(p << 16); }
__device__ __forceinline__ float bf_hi(u32 p) { return __uint_as_float(p & 0xffff0000u); }
__device__ __forceinline__ float rbf(float x) { return bf_lo(pack_bf16(x, x)); }
__device__ __forceinline__ u32 perm_lo(u32 a, u32 b) { return __builtin_amdgcn_perm(b, a, 0x05040100u); }
__device__ __forceinline__ u32 perm_hi(u32 a, u32 b) { return __builtin_amdgcn_perm(b, a, 0x07060302u); }
__device__ __forceinline__ float rdlane(float x, int l) { return __builtin_bit_cast(float, __builtin_amdgcn_readlane(__builtin_bit_cast(int, x), l)); }

template <int CTRL> __device__ __forceinline__ float dppf(float x) {
  return __builtin_bit_cast(float, __builtin_amdgcn_mov_dpp(__builtin_bit_cast(int, x), CTRL, 0xf, 0xf, true));
}
constexpr int XOR1 = 0xB1, XOR2 = 0x4E, XOR7 = 0x141, ROR8 = 0x128, ROR4 = 0x124, ROR2 = 0x122, ROR1 = 0x121;
constexpr u32 ONES = 0x3f803f80u;

__device__ __forceinline__ float tadd32(float a, float b) {
  auto s = __builtin_amdgcn_permlane32_swap(__float_as_uint(a), __float_as_uint(b), false, false);
  return __uint_as_float(s[0]) + __uint_as_float(s[1]);
}
__device__ __forceinline__ float tadd16(float a, float b) {
  auto s = __builtin_amdgcn_permlane16_swap(__float_as_uint(a), __float_as_uint(b), false, false);
  return __uint_as_float(s[0]) + __uint_as_float(s[1]);
}
__device__ __forceinline__ float vmax(float a, float b) { float r; asm("v_max_f32 %0, %1, %2" : "=v"(r) : "v"(a), "v"(b)); return r; }
__device__ __forceinline__ float vmax3(float a, float b, float c) { float r; asm("v_max3_f32 %0, %1, %2, %3" : "=v"(r) : "v"(a), "v"(b), "v"(c)); return r; }
__device__ __forceinline__ float tmax32(float a) {
  auto s = __builtin_amdgcn_permlane32_swap(__float_as_uint(a), __float_as_uint(a), false, false);
  return vmax(__uint_as_float(s[0]), __uint_as_float(s[1]));
}
__device__ __forceinline__ float tmax16(float a) {
  auto s = __builtin_amdgcn_permlane16_swap(__float_as_uint(a), __float_as_uint(a), false, false);
  return vmax(__uint_as_float(s[0]), __uint_as_float(s[1]));
}
__device__ __forceinline__ float rowsum16(float x) {
  x += dppf<ROR8>(x); x += dppf<ROR4>(x); x += dppf<ROR2>(x); x += dppf<ROR1>(x);
  return x;
}
__device__ __forceinline__ float reduce8(const float (&a)[8], int lane) {
  float t4[4], t2[2];
#pragma unroll
  for (int c = 0; c < 4; ++c) t4[c] = tadd32(a[c], a[c + 4]);
#pragma unroll
  for (int c = 0; c < 2; ++c) t2[c] = tadd16(t4[c], t4[c + 2]);
  const float lo = t2[0] + dppf<ROR8>(t2[0]), hi = t2[1] + dppf<ROR8>(t2[1]);
  float r = (lane & 8) ? hi : lo;
  r += dppf<XOR7>(r); r += dppf<XOR2>(r); r += dppf<XOR1>(r);
  return r;
}

template <int N>
__device__ __forceinline__ int gather(__amdgpu_buffer_rsrc_t rs, const u32 (&off)[N], u32 tag, v4u (&v)[N], bool& dead, bool slow = false) {
  int spin = 0;
  for (;; ++spin) {
    asm volatile("" ::: "memory");
#pragma unroll
    for (int i = 0; i < N; ++i) v[i] = __builtin_amdgcn_raw_buffer_load_b128(rs, off[i], 0, 16);
    bool ok = true;
#pragma unroll
    for (int i = 0; i < N; ++i) ok &= (v[i].y == tag) & (v[i].w == tag);
    if (__all(ok)) break;
    if (dead || spin > SPIN_CAP) { dead = true; break; }
    if (slow) __builtin_amdgcn_s_sleep(D_BACK);
  }
  return spin;
}

struct Args {
  const u16* x; const u16* kc; const u16* vc; const u16* wq; const u16* wo;
  const u16* w1; const u16* b1; const u16* w2; const u16* b2;
  u16* out; unsigned char* ws;
};

#define STAMP(i) do {} while (0)
#define STAMPN(i, n) do { (void)(n); } while (0)

#define TR16(t, ra, o0, o1, o2, o3, o4, o5, o6, o7, o8, o9, o10, o11, o12, o13, o14, o15)                                                                   \
  asm volatile("ds_read_b64_tr_b16 %0, %16 offset:" #o0 "\n\tds_read_b64_tr_b16 %1, %16 offset:" #o1 "\n\tds_read_b64_tr_b16 %2, %16 offset:" #o2                \
               "\n\tds_read_b64_tr_b16 %3, %16 offset:" #o3 "\n\tds_read_b64_tr_b16 %4, %16 offset:" #o4 "\n\tds_read_b64_tr_b16 %5, %16 offset:" #o5            \
               "\n\tds_read_b64_tr_b16 %6, %16 offset:" #o6 "\n\tds_read_b64_tr_b16 %7, %16 offset:" #o7 "\n\tds_read_b64_tr_b16 %8, %16 offset:" #o8            \
               "\n\tds_read_b64_tr_b16 %9, %16 offset:" #o9 "\n\tds_read_b64_tr_b16 %10, %16 offset:" #o10 "\n\tds_read_b64_tr_b16 %11, %16 offset:" #o11        \
               "\n\tds_read_b64_tr_b16 %12, %16 offset:" #o12 "\n\tds_read_b64_tr_b16 %13, %16 offset:" #o13 "\n\tds_read_b64_tr_b16 %14, %16 offset:" #o14      \
               "\n\tds_read_b64_tr_b16 %15, %16 offset:" #o15 "\n\ts_waitcnt lgkmcnt(0)"                                                                         \
               : "=&v"(t[0]), "=&v"(t[1]), "=&v"(t[2]), "=&v"(t[3]), "=&v"(t[4]), "=&v"(t[5]), "=&v"(t[6]), "=&v"(t[7]), "=&v"(t[8]), "=&v"(t[9]), "=&v"(t[10]), \
                 "=&v"(t[11]), "=&v"(t[12]), "=&v"(t[13]), "=&v"(t[14]), "=&v"(t[15])                                                                            \
               : "v"(ra) : "memory")

#define MFMA32(A, B, C) __builtin_amdgcn_mfma_f32_16x16x32_bf16(__builtin_bit_cast(v8bf, A), __builtin_bit_cast(v8bf, B), C, 0, 0, 0)

__device__ __forceinline__ float ln_gemv4(const v4u xv, const v4u (&w)[4], float cs) {
  float a0 = 0.f, a1 = 0.f, a2 = 0.f, a3 = 0.f, s1 = 0.f, s2 = 0.f;
#pragma unroll
  for (int p = 0; p < 4; ++p) {
    a0 = dot2(xv[p], w[p].x, a0); a1 = dot2(xv[p], w[p].y, a1); a2 = dot2(xv[p], w[p].z, a2); a3 = dot2(xv[p], w[p].w, a3);
    s1 = dot2(xv[p], ONES, s1); s2 = dot2(xv[p], xv[p], s2);
  }
  const float acc = rowsum16(tadd16(tadd32(a0, a2), tadd32(a1, a3)));
  float ss = tadd32(s1, s2); ss = tadd16(ss, ss); ss = rowsum16(ss);
  const float mean = rdlane(ss, 0) * (1.f / 512.f);
  const float var = fmaxf(rdlane(ss, 32) * (1.f / 512.f) - mean * mean, 0.f);
  const float rstd = __builtin_amdgcn_rsqf(var + 1e-5f);
  return (acc - mean * cs) * rstd;
}

__global__ void __launch_bounds__(NTHR, 2) fused_decode(Args a) {
  __shared__ __attribute__((aligned(16))) unsigned char smem[L_END];
  LAS unsigned char* L = (LAS unsigned char*)smem;
  const u32 lbase = (u32)(size_t)L;
  const int tid = threadIdx.x, lane = tid & 63, wave = __builtin_amdgcn_readfirstlane(tid >> 6);
  const int b = blockIdx.x, g = b & 7, j = b >> 3;

  const bool isA = j < 16, isR = j < 4, isC = (j >= 16) & (j < 24), isH = (j >= 24) & (j < 31);
  const int jj = j & 15;
  const int qkh = (j >> 3) & 1, qcg = j & 7;
  const int ci = j & 7;
  const int hidx = g * 7 + (isH ? j - 24 : 0);
  const int ri = 4 * g + (j & 3);

  {
    u64 pc; asm volatile("s_getpc_b64 %0" : "=s"(pc));
    if (wave >= 1 && wave <= 3) {
      const u32 o = (u32)(wave - 1) * 8192u + (u32)lane * 128u;
      dma16_keep((const char*)((pc & ~127ull) + (o < CODE_BYTES ? o : 0u)), lbase + L_SCR + (wave - 1) * 1024);
    }
  }
  u32 epoch;
  asm volatile("s_load_dword %0, %1, 0x0" : "=s"(epoch) : "s"(a.ws) : "memory");
  auto kv_issue = [&](int t0, int t1) {
    int ln = tid & 63; asm volatile("" : "+v"(ln));
    const int w4 = wave - 4;
    const size_t kvoff = ((size_t)g * SK + (SK - WIN) + jj * 256) * 256;
    const int r0 = w4 * 4 + (ln >> 4);
    const char* kp = (const char*)a.kc + kvoff + r0 * 256 + ((ln & 15) ^ r0) * 16;
    const char* vp = (const char*)a.vc + kvoff + r0 * 256 + ((ln & 15) ^ ((r0 & 7) << 1)) * 16 - 65536;
#pragma unroll 1
    for (int t = t0; t < t1; ++t) {
      asm volatile("s_waitcnt vmcnt(%0)" :: "n"(KV_WIN - 1) : "memory");
      dma16((t < 16 ? kp : vp) + t * 4096, lbase + (t * 4 + w4) * 1024);
    }
  };
  if (isA) {
    if (wave >= 4) { if (D_KV0) __builtin_amdgcn_s_sleep(D_KV0); kv_issue(0, KV_SPLIT); }
  } else {
#pragma unroll
    for (int t = 0; t < 4; ++t) {
      const int lp = wave * 8 + (lane >> 3);
      dma16((const char*)a.wq + (size_t)(256 * qkh + 4 * lp + t) * 8192 + g * 1024 + qcg * 128 + ((lane & 7) ^ ((lp >> 1) & 7)) * 16, lbase + L_W4 + (t * 8 + wave) * 1024);
    }
  }
  if (wave == 0) dma16((const char*)a.x + lane * 16, lbase + L_XS);
  if (wave == 1) {
    const char* src = (lane & 7) < 4 ? (const char*)a.b1 + hidx * 64 + (lane & 3) * 16 : (const char*)a.b2 + ri * 32 + (lane & 1) * 16;
    dma16(src, lbase + L_B);
  }

  bool dead = false;
  const int gq = lane >> 4, hq = lane & 3, kr = lane & 15;
  v4u RA[8], RB[8], RH[4];
  float r_cs = 0.f, h_cs = 0.f, h_bias = 0.f;
  u32 xkeep = 0u;
#pragma unroll
  for (int i = 0; i < 8; ++i) { RA[i] = (v4u){0u, 0u, 0u, 0u}; RB[i] = (v4u){0u, 0u, 0u, 0u}; }
#pragma unroll
  for (int i = 0; i < 4; ++i) RH[i] = (v4u){0u, 0u, 0u, 0u};

  auto prep4 = [&](u32 base, v4u& w0, v4u& w1, v4u& w2, v4u& w3) -> float {
    int ln = tid & 63; asm volatile("" : "+v"(ln));
    v2u r[8];
#pragma unroll
    for (int i = 0; i < 8; ++i) r[i] = *(const LAS v2u*)(L + base + (i * 64 + ln) * 64 + ((wave >> 1) ^ ((ln >> 2) & 3)) * 16 + (wave & 1) * 8);
    v4u w[4];
#pragma unroll
    for (int p = 0; p < 4; ++p)
      w[p] = (v4u){perm_lo(r[2 * p].x, r[2 * p + 1].x), perm_hi(r[2 * p].x, r[2 * p + 1].x), perm_lo(r[2 * p].y, r[2 * p + 1].y), perm_hi(r[2 * p].y, r[2 * p + 1].y)};
    float c0 = 0.f, c1 = 0.f, c2 = 0.f, c3 = 0.f;
#pragma unroll
    for (int p = 0; p < 4; ++p) { c0 = dot2(w[p].x, ONES, c0); c1 = dot2(w[p].y, ONES, c1); c2 = dot2(w[p].z, ONES, c2); c3 = dot2(w[p].w, ONES, c3); }
    w0 = w[0]; w1 = w[1]; w2 = w[2]; w3 = w[3];
    return rowsum16(tadd16(tadd32(c0, c2), tadd32(c1, c3)));
  };
  auto tr_addr = [&](int c, int half) -> u32 {
    int ln = tid & 63; asm volatile("" : "+v"(ln));
    const int row = 8 * (ln >> 4) + 4 * half + ((ln & 15) >> 2), p = ln & 3;
    return lbase + L_WT + row * 1024 + (((8 * wave + 2 * c + (p >> 1)) ^ ((row & 7) << 1)) * 16) + (p & 1) * 8;
  };
  asm volatile("s_waitcnt lgkmcnt(0)" : "+s"(epoch) :: "memory");
  const u32 tag = epoch + 1u;
  const __amdgpu_buffer_rsrc_t rs = __builtin_amdgcn_make_buffer_rsrc(a.ws + X_BASE + (epoch % X_NREG) * X_REGION, 0, X_REGION, 0x00020000);
  STAMP(1);

  auto store_partial = [&](v4f c0, v4f c1, v4f c2, v4f c3, u32 base, bool act) {
    float v[4];
#pragma unroll
    for (int r = 0; r < 4; ++r) {
      const float lo = (lane & 1) ? c1[r] : c0[r], hi = (lane & 1) ? c3[r] : c2[r];
      v[r] = (lane & 2) ? hi : lo;
    }
    if (act && kr < 4) {
      const u32 o = base + (u32)(wave * 4 + hq) * 128 + gq * 32;
      __builtin_amdgcn_raw_buffer_store_b128((v4u){__float_as_uint(v[0]), tag, __float_as_uint(v[1]), tag}, rs, o, 0, 16);
      __builtin_amdgcn_raw_buffer_store_b128((v4u){__float_as_uint(v[2]), tag, __float_as_uint(v[3]), tag}, rs, o + 16, 0, 16);
    }
  };

  auto stage_q = [&](u32 xl) {
    const v2u xo = *(const LAS v2u*)(L + L_XS + qkh * 512 + lane * 8), xq = *(const LAS v2u*)(L + L_XS + (qkh ^ 1) * 512 + lane * 8);
    float ac[8];
#pragma unroll
    for (int q = 0; q < 8; ++q) ac[q] = dot2(xo.y, RH[2 + (q >> 2)][q & 3], dot2(xo.x, RH[q >> 2][q & 3], 0.f));
    const float s1 = dot2(xq.y, ONES, dot2(xq.x, ONES, dot2(xo.y, ONES, dot2(xo.x, ONES, 0.f))));
    const float s2 = dot2(xq.y, xq.y, dot2(xq.x, xq.x, dot2(xo.y, xo.y, dot2(xo.x, xo.x, 0.f))));
    const float acc = reduce8(ac, lane);
    float ss = tadd32(s1, s2); ss = tadd16(ss, ss); ss = rowsum16(ss);
    const float mean = rdlane(ss, 0) * (1.f / 512.f);
    const float var = fmaxf(rdlane(ss, 32) * (1.f / 512.f) - mean * mean, 0.f);
    const float y = (acc - mean * r_cs) * __builtin_amdgcn_rsqf(var + 1e-5f);
    if (!isA && (lane & 7) == 0) {
      const v2u d = {__float_as_uint(y), tag};
      __builtin_amdgcn_raw_buffer_store_b64(d, rs, xl + X_E1 + (u32)(g * 16 + jj) * 512 + wave * 64 + lane, 0, 16);
    }
  };

  if (!isA) {
    asm volatile("s_waitcnt vmcnt(0)" ::: "memory");
    lds_barrier();
    v4u r[4];
#pragma unroll
    for (int i = 0; i < 4; ++i) r[i] = *(const LAS v4u*)(L + L_W4 + (i * 64 + lane) * 128 + (wave ^ ((lane >> 1) & 7)) * 16);
#pragma unroll
    for (int p = 0; p < 2; ++p) {
      RH[2 * p] = (v4u){perm_lo(r[2 * p].x, r[2 * p + 1].x), perm_hi(r[2 * p].x, r[2 * p + 1].x), perm_lo(r[2 * p].y, r[2 * p + 1].y), perm_hi(r[2 * p].y, r[2 * p + 1].y)};
      RH[2 * p + 1] = (v4u){perm_lo(r[2 * p].z, r[2 * p + 1].z), perm_hi(r[2 * p].z, r[2 * p + 1].z), perm_lo(r[2 * p].w, r[2 * p + 1].w), perm_hi(r[2 * p].w, r[2 * p + 1].w)};
    }
    float c[8];
#pragma unroll
    for (int q = 0; q < 8; ++q) c[q] = dot2(RH[2 + (q >> 2)][q & 3], ONES, dot2(RH[q >> 2][q & 3], ONES, 0.f));
    r_cs = reduce8(c, lane);
    STAMP(2);
    stage_q(0u);
    STAMP(3);
  }
  {

      const v4u z = {0u, ~tag, 0u, ~tag};
      int t_ = tid; asm volatile("" : "+v"(t_));
      if (isA) {
      } else {
        if ((PREW & 2) && (isC | isH)) __builtin_amdgcn_raw_buffer_store_b128(z, rs, (isC ? X_E3 + (u32)(g * 8 + ci) * 4096 : X_E5 + (u32)hidx * 4096) + (t_ >> 8) * X_LAYER + (t_ & 255) * 16, 0, 16);
        if ((PREW & 2) && t_ < 32) __builtin_amdgcn_raw_buffer_store_b128(z, rs, X_LAYER + X_E1 + (u32)(g * 16 + jj) * 512 + t_ * 16, 0, 16);
        if (isC | isH) {
          lds_barrier();
          int ln = tid & 63; asm volatile("" : "+v"(ln));
          if (isC) {
            __builtin_amdgcn_s_sleep(D_WO);
            const char* p = (const char*)a.wo + (size_t)(g * 512 + ci * 64 + wave) * 1024 + (ln ^ (wave << 1)) * 16;
#pragma unroll
            for (int t = 0; t < 8; ++t) dma16(p + t * 8192, lbase + L_WT + (t * 8 + wave) * 1024);
          } else {
            __builtin_amdgcn_s_sleep(D_W12);
#pragma unroll
            for (int t = 0; t < 4; ++t) {
              const int tt = (t + 2 * (hidx & 1)) & 3, lp = tt * 16 + (ln >> 2);
              dma16((const char*)a.w1 + (size_t)(8 * lp + wave) * 3584 + hidx * 64 + ((ln & 3) ^ ((lp >> 2) & 3)) * 16, lbase + L_W1 + (wave * 4 + tt) * 1024);
            }
            const char* p = (const char*)a.w2 + (size_t)(hidx * 32 + wave) * 1024 + (ln ^ (wave << 1)) * 16;
#pragma unroll
            for (int t = 0; t < 4; ++t) dma16(p + t * 8192, lbase + L_WT + (t * 8 + wave) * 1024);
          }
          asm volatile("s_waitcnt vmcnt(0)" ::: "memory");
          STAMP(15);
          lds_barrier();
          {
            v2u t[16];
            const u32 a0 = tr_addr(0, 0), a1 = tr_addr(0, 1), a2 = tr_addr(1, 0), a3 = tr_addr(1, 1), a4 = tr_addr(2, 0), a5 = tr_addr(2, 1), a6 = tr_addr(3, 0), a7 = tr_addr(3, 1);
            asm volatile(
                "ds_read_b64_tr_b16 %0, %16\n\tds_read_b64_tr_b16 %1, %17\n\tds_read_b64_tr_b16 %2, %16 offset:32768\n\tds_read_b64_tr_b16 %3, %17 offset:32768\n\t"
                "ds_read_b64_tr_b16 %4, %18\n\tds_read_b64_tr_b16 %5, %19\n\tds_read_b64_tr_b16 %6, %18 offset:32768\n\tds_read_b64_tr_b16 %7, %19 offset:32768\n\t"
                "ds_read_b64_tr_b16 %8, %20\n\tds_read_b64_tr_b16 %9, %21\n\tds_read_b64_tr_b16 %10, %20 offset:32768\n\tds_read_b64_tr_b16 %11, %21 offset:32768\n\t"
                "ds_read_b64_tr_b16 %12, %22\n\tds_read_b64_tr_b16 %13, %23\n\tds_read_b64_tr_b16 %14, %22 offset:32768\n\tds_read_b64_tr_b16 %15, %23 offset:32768\n\t"
                "s_waitcnt lgkmcnt(0)"
                : "=&v"(t[0]), "=&v"(t[1]), "=&v"(t[2]), "=&v"(t[3]), "=&v"(t[4]), "=&v"(t[5]), "=&v"(t[6]), "=&v"(t[7]), "=&v"(t[8]), "=&v"(t[9]), "=&v"(t[10]),
                  "=&v"(t[11]), "=&v"(t[12]), "=&v"(t[13]), "=&v"(t[14]), "=&v"(t[15])
                : "v"(a0), "v"(a1), "v"(a2), "v"(a3), "v"(a4), "v"(a5), "v"(a6), "v"(a7) : "memory");
#pragma unroll
            for (int c = 0; c < 4; ++c) {
              RB[2 * c] = (v4u){t[4 * c].x, t[4 * c].y, t[4 * c + 1].x, t[4 * c + 1].y};
              RB[2 * c + 1] = (v4u){t[4 * c + 2].x, t[4 * c + 2].y, t[4 * c + 3].x, t[4 * c + 3].y};
            }
          }
          if (isH) {
            h_cs = prep4(L_W1, RA[0], RA[1], RA[2], RA[3]);
            h_bias = bf_lo(*(const LAS u16*)(L + L_B + (wave * 4 + (ln >> 4)) * 2));
          }
          STAMP(28);
        }
      }
      }

  for (int layer = 0; layer < 2; ++layer) {
    const u32 xl = layer * X_LAYER;
    if (layer != 0) {
      if (wave == 0) {
        __builtin_amdgcn_s_sleep(D_E6);
        const u32 o0 = X_E6 + (lane >> 1) * 128 + (lane & 1) * 32;
        const u32 off[2] = {o0, o0 + 16};
        v4u v[2];
        const int n = gather<2>(rs, off, tag, v, dead);
        STAMPN(16, n);
        *(LAS v4u*)(L + L_XS + lane * 16) = (v4u){v[0].x, v[0].z, v[1].x, v[1].z};
      }
      lds_barrier();
      stage_q(X_LAYER);
      STAMP(17);
    }

    if (layer != 0 || isA) {
      if (layer != 0) lds_barrier();
      if (layer == 0) {
        if (wave < 4) {
          __builtin_amdgcn_s_sleep(D_Q0);
          const u32 o0 = xl + X_E1 + (u32)(g * 64 + (lane >> 5) * 32 + wave * 8) * 128 + (lane & 31) * 16;
          const u32 off[2] = {o0, o0 + 512};
          v4u v[2];
          const int n = gather<2>(rs, off, tag, v, dead, true);
          STAMPN(4, n);
#pragma unroll
          for (int i = 0; i < 2; ++i) {
            float pA = __uint_as_float(v[i].x), pB = __uint_as_float(v[i].z);
            pA = tadd32(pA, pA); pB = tadd32(pB, pB);
            if (lane < 32) *(LAS u32*)(L + L_QS + (2 * i + (lane >> 4)) * 256 + wave * 64 + (lane & 15) * 4) = pack_bf16(pA, pB);
          }
        } else {
          kv_issue(KV_SPLIT, 32);
          STAMP(4);
        }
      } else {
        __builtin_amdgcn_s_sleep(D_E1);
        const u32 off[1] = {xl + X_E1 + (u32)(g * 64 + (lane >> 5) * 32 + wave * 4) * 128 + (lane & 31) * 16};
        v4u v[1];
        const int n = gather<1>(rs, off, tag, v, dead);
        STAMPN(18, n);
        float pA = __uint_as_float(v[0].x), pB = __uint_as_float(v[0].z);
        pA = tadd32(pA, pA); pB = tadd32(pB, pB);
        if (lane < 32) *(LAS u32*)(L + L_QS + (2 * (wave & 1) + (lane >> 4)) * 256 + (wave >> 1) * 64 + (lane & 15) * 4) = pack_bf16(pA, pB);
      }
      lds_barrier();
      if (layer == 0) {
#pragma unroll
        for (int s = 0; s < 8; ++s) RA[s] = *(const LAS v4u*)(L + L_K + (wave * 32 + (s >> 2) * 16 + kr) * 256 + (((4 * (s & 3) + gq) ^ kr) * 16));
      }
      v4f sa = {0.f, 0.f, 0.f, 0.f}, sb = {0.f, 0.f, 0.f, 0.f};
      {
        v4u qf[4];
#pragma unroll
        for (int s = 0; s < 4; ++s) qf[s] = *(const LAS v4u*)(L + L_QS + s * 256 + hq * 64 + gq * 16);
#pragma unroll
        for (int s = 0; s < 4; ++s) { sa = MFMA32(RA[s], qf[s], sa); sb = MFMA32(RA[4 + s], qf[s], sb); }
      }
      constexpr float C2 = 0.08838834764831845f * 1.4426950408889634f;
      sa *= C2; sb *= C2;
      if (layer == 0) {
        if (wave >= 4) asm volatile("s_waitcnt vmcnt(0)" ::: "memory");
        STAMP(14);
        lds_barrier();
        const int row0 = wave * 32 + 4 * gq + ((lane & 15) >> 2), vp = lane & 3;
        const u32 va = lbase + L_V + row0 * 256 + ((row0 & 7) << 5) + (vp >> 1) * 16 + (vp & 1) * 8;
        v2u t[16];
        asm volatile(
            "ds_read_b64_tr_b16 %0, %16\n\tds_read_b64_tr_b16 %1, %16 offset:4096\n\tds_read_b64_tr_b16 %2, %17\n\tds_read_b64_tr_b16 %3, %17 offset:4096\n\t"
            "ds_read_b64_tr_b16 %4, %18\n\tds_read_b64_tr_b16 %5, %18 offset:4096\n\tds_read_b64_tr_b16 %6, %19\n\tds_read_b64_tr_b16 %7, %19 offset:4096\n\t"
            "ds_read_b64_tr_b16 %8, %20\n\tds_read_b64_tr_b16 %9, %20 offset:4096\n\tds_read_b64_tr_b16 %10, %21\n\tds_read_b64_tr_b16 %11, %21 offset:4096\n\t"
            "ds_read_b64_tr_b16 %12, %22\n\tds_read_b64_tr_b16 %13, %22 offset:4096\n\tds_read_b64_tr_b16 %14, %23\n\tds_read_b64_tr_b16 %15, %23 offset:4096\n\t"
            "s_waitcnt lgkmcnt(0)"
            : "=&v"(t[0]), "=&v"(t[1]), "=&v"(t[2]), "=&v"(t[3]), "=&v"(t[4]), "=&v"(t[5]), "=&v"(t[6]), "=&v"(t[7]), "=&v"(t[8]), "=&v"(t[9]), "=&v"(t[10]),
              "=&v"(t[11]), "=&v"(t[12]), "=&v"(t[13]), "=&v"(t[14]), "=&v"(t[15])
            : "v"(va), "v"(va ^ 32u), "v"(va ^ 64u), "v"(va ^ 96u), "v"(va ^ 128u), "v"(va ^ 160u), "v"(va ^ 192u), "v"(va ^ 224u) : "memory");
#pragma unroll
        for (int u = 0; u < 8; ++u) RB[u] = (v4u){t[2 * u].x, t[2 * u].y, t[2 * u + 1].x, t[2 * u + 1].y};
      }
      float ref = 0.f;
      for (int pass = 0;; ++pass) {
        float p[8];
#pragma unroll
        for (int r = 0; r < 4; ++r) { p[r] = __builtin_amdgcn_exp2f(sa[r] - ref); p[4 + r] = __builtin_amdgcn_exp2f(sb[r] - ref); }
        const v4u pf = {pack_bf16(p[0], p[1]), pack_bf16(p[2], p[3]), pack_bf16(p[4], p[5]), pack_bf16(p[6], p[7])};
        v4f oa = {0.f, 0.f, 0.f, 0.f}, ob = {0.f, 0.f, 0.f, 0.f};
#pragma unroll
        for (int c = 0; c < 4; ++c) {
          const bool on = (kr >> 2) == c;
          const v4u pm = {on ? pf.x : 0u, on ? pf.y : 0u, on ? pf.z : 0u, on ? pf.w : 0u};
          oa = MFMA32(pm, RB[c], oa); ob = MFMA32(pm, RB[4 + c], ob);
        }
        float m = vmax3(vmax3(sa[0], sa[1], sa[2]), vmax3(sa[3], sb[0], sb[1]), vmax(sb[2], sb[3]));
        float ls = dot2(pf.w, ONES, dot2(pf.z, ONES, 0.f)) + dot2(pf.y, ONES, dot2(pf.x, ONES, 0.f));
        m = tmax16(m); ls = tadd16(ls, ls); m = tmax32(m); ls = tadd32(ls, ls);
        if (lane < 4) *(LAS v2u*)(L + L_ML + hq * 64 + wave * 8) = (v2u){__float_as_uint(m), __float_as_uint(ls)};
        *(LAS v4f*)(L + L_OW + wave * 2048 + lane * 16) = oa;
        *(LAS v4f*)(L + L_OW + wave * 2048 + 1024 + lane * 16) = ob;
        lds_barrier();
        const v4f x0 = *(const LAS v4f*)(L + L_OW + gq * 2048 + wave * 256 + kr * 16), x1 = *(const LAS v4f*)(L + L_OW + 8192 + gq * 2048 + wave * 256 + kr * 16);
        const v2u ml = *(const LAS v2u*)(L + L_ML + (lane & 31) * 8);
        const float mw = __uint_as_float(ml.x);
        if (isA && pass == 0 && !__all(fabsf(mw) <= SAFE_RANGE)) {
          const v4u q0 = *(const LAS v4u*)(L + L_ML + hq * 64), q1 = *(const LAS v4u*)(L + L_ML + hq * 64 + 16);
          const v4u q2 = *(const LAS v4u*)(L + L_ML + hq * 64 + 32), q3 = *(const LAS v4u*)(L + L_ML + hq * 64 + 48);
          ref = fmaxf(fmaxf(fmaxf(__uint_as_float(q0.x), __uint_as_float(q0.z)), fmaxf(__uint_as_float(q1.x), __uint_as_float(q1.z))),
                      fmaxf(fmaxf(__uint_as_float(q2.x), __uint_as_float(q2.z)), fmaxf(__uint_as_float(q3.x), __uint_as_float(q3.z))));
          lds_barrier();
          continue;
        }
        const v4f xs = x0 + x1;
        const float o = tadd16(tadd32(xs[0], xs[2]), tadd32(xs[1], xs[3]));
        const float o1 = dppf<XOR1>(o);
        const u32 pr = pack_bf16(o, o1);
        const u32 pr2 = (u32)__builtin_amdgcn_mov_dpp((int)pr, XOR2, 0xf, 0xf, true);
        const u32 pbase = xl + X_E2 + (u32)((g * 16 + jj) * 17) * 128;
        if (isA && (lane & 3) == 0) {
          const v4u d = {pr, tag, pr2, tag};
          __builtin_amdgcn_raw_buffer_store_b128(d, rs, pbase + (gq * 4 + (wave >> 1)) * 128 + ((wave & 1) * 4 + (kr >> 2)) * 16, 0, 16);
        }
        if (wave == 7) {
          float lt = __uint_as_float(ml.y);
          lt += dppf<XOR1>(lt); lt += dppf<XOR2>(lt); lt += dppf<XOR7>(lt);
          const float rh = __builtin_bit_cast(float, __builtin_amdgcn_ds_bpermute((lane >> 3) * 4, __builtin_bit_cast(int, ref)));
          if (isA && lane < 32 && (lane & 7) == 0) {
            const v4u d = {__float_as_uint(rh), tag, __float_as_uint(lt), tag};
            __builtin_amdgcn_raw_buffer_store_b128(d, rs, pbase + 16 * 128 + (lane >> 3) * 16, 0, 16);
          }
        }
        break;
      }
      STAMP(5 + layer * 14);
      if (layer == 0 && wave >= 1 && wave < 4) {
        const v4u z = {0u, ~tag, 0u, ~tag};
        int pc = tid - 64; asm volatile("" : "+v"(pc));
        if (pc < 136) __builtin_amdgcn_raw_buffer_store_b128(z, rs, X_LAYER + X_E2 + (u32)((g * 16 + jj) * 17) * 128 + pc * 16, 0, 16);
        else if (isR && pc < 160) { const int line = (pc - 136) >> 3;
          __builtin_amdgcn_raw_buffer_store_b128(z, rs, (line < 2 ? line * X_LAYER + X_E4 : X_E6) + (u32)ri * 128 + (pc & 7) * 16, 0, 16); }
      }
    }

    {
      if (wave == 0) {
        __builtin_amdgcn_s_sleep(D_E2);
        if (layer == 0 && !isA && !isC && !isH) __builtin_amdgcn_s_sleep(D_G0);
        u32 off[8];
#pragma unroll
        for (int i = 0; i < 4; ++i) {
          const u32 pb = xl + X_E2 + (u32)((g * 16 + 4 * i + gq) * 17) * 128;
          off[i] = pb + ci * 256 + kr * 16;
          off[4 + i] = pb + 16 * 128 + (ci >> 1) * 16;
        }
        v4u v[8];
        const int n = gather<8>(rs, off, tag, v, dead, layer == 0);
        STAMPN(6 + layer * 14, n);
        float M = vmax(vmax3(__uint_as_float(v[4].x), __uint_as_float(v[5].x), __uint_as_float(v[6].x)), __uint_as_float(v[7].x));
        M = tmax16(M); M = tmax32(M);
        float a0 = 0.f, a1 = 0.f, a2 = 0.f, a3 = 0.f, Ls = 0.f;
#pragma unroll
        for (int i = 0; i < 4; ++i) {
          const float e = __builtin_amdgcn_exp2f(__uint_as_float(v[4 + i].x) - M);
          Ls = fmaf(e, __uint_as_float(v[4 + i].z), Ls);
          a0 = fmaf(e, bf_lo(v[i].x), a0); a1 = fmaf(e, bf_hi(v[i].x), a1); a2 = fmaf(e, bf_lo(v[i].z), a2); a3 = fmaf(e, bf_hi(v[i].z), a3);
        }
        float b0 = tadd32(a0, a2), b1 = tadd32(a1, a3); Ls = tadd32(Ls, Ls);
        b0 = tadd16(b0, b0); b1 = tadd16(b1, b1); Ls = tadd16(Ls, Ls);
        const float inv = __builtin_amdgcn_rcpf(Ls);
        if (!(lane & 16)) *(LAS u32*)(L + L_OS + kr * 8 + (lane >> 5) * 4) = pack_bf16(b0 * inv, b1 * inv);
      }
      lds_barrier();
      const v4u of0 = *(const LAS v4u*)(L + L_OS + gq * 16), of1 = *(const LAS v4u*)(L + L_OS + 64 + gq * 16);
      v4f acc[4];
#pragma unroll
      for (int c = 0; c < 4; ++c) acc[c] = MFMA32(RB[2 * c + 1], of1, MFMA32(RB[2 * c], of0, ((v4f){0.f, 0.f, 0.f, 0.f})));
      store_partial(acc[0], acc[1], acc[2], acc[3], xl + X_E3 + (u32)(g * 8 + ci) * 4096, isC);
      STAMP(7 + layer * 14);
    }

#pragma unroll
    for (int ph = 0; ph < 2; ++ph) {
      lds_barrier();
      if (wave == 0) {
        constexpr int NP = 8;
        if (ph == 0) __builtin_amdgcn_s_sleep(D_E3); else __builtin_amdgcn_s_sleep(D_E5);
        u32 off[NP];
#pragma unroll
        for (int i = 0; i < NP; ++i) off[i] = xl + (ph == 0 ? X_E3 : X_E5) + (u32)(((ph == 1 && i == 7 ? 6 : i) * 8 + (lane >> 3)) * 32 + ri) * 128 + (lane & 7) * 16;
        const u32 xin = ph == 0 ? *(const LAS u32*)(L + L_XS + ri * 32 + (lane & 7) * 4) : xkeep;
        const u32 bb = *(const LAS u32*)(L + L_B + 64 + (lane & 7) * 4);
        v4u v[NP];
        const int n = gather<NP>(rs, off, tag, v, dead);
        STAMPN(8 + ph * 4 + layer * 14, n);
        float s0 = 0.f, s1 = 0.f;
#pragma unroll
        for (int i = 0; i < (ph == 0 ? 8 : 7); ++i) { s0 += __uint_as_float(v[i].x); s1 += __uint_as_float(v[i].z); }
        float t = tadd32(s0, s1); t = tadd16(t, t); t += dppf<ROR8>(t);
        float r = rbf(t);
        if (ph == 1) r = rbf(r + ((lane & 32) ? bf_hi(bb) : bf_lo(bb)));
        r = r + ((lane & 32) ? bf_hi(xin) : bf_lo(xin));
        auto sw = __builtin_amdgcn_permlane32_swap(__float_as_uint(r), __float_as_uint(r), false, false);
        const u32 pr = pack_bf16(__uint_as_float(sw[0]), __uint_as_float(sw[1]));
        xkeep = pr;
        if (isR && lane < 8) {
          if (ph == 0 || layer == 0) {
            const v2u d = {pr, tag};
            __builtin_amdgcn_raw_buffer_store_b64(d, rs, xl + (ph == 0 ? X_E4 : X_E6) + (u32)ri * 128 + lane * 8, 0, 16);
          } else {
            *(u32*)(a.out + ri * 16 + lane * 2) = pr;
          }
        }
        STAMP(9 + ph * 4 + layer * 14);
      }
      if (ph == 0) {
        if (wave == 0) {
          __builtin_amdgcn_s_sleep(D_E4);
          const u32 o0 = xl + X_E4 + (lane >> 1) * 128 + (lane & 1) * 32;
          const u32 off[2] = {o0, o0 + 16};
          v4u v[2];
          const int n = gather<2>(rs, off, tag, v, dead);
          STAMPN(10 + layer * 14, n);
          *(LAS v4u*)(L + L_XP + lane * 16) = (v4u){v[0].x, v[0].z, v[1].x, v[1].z};
        }
        lds_barrier();
        const v4u xv = *(const LAS v4u*)(L + L_XP + lane * 16);
        const v4u w1r[4] = {RA[0], RA[1], RA[2], RA[3]};
        const float z = rbf(rbf(ln_gemv4(xv, w1r, h_cs)) + h_bias);
        const float u2 = z * (1.5957691216057308f + 0.07135481627260025f * z * z);
        const float y = z * __builtin_amdgcn_rcpf(1.f + __builtin_amdgcn_exp2f(-1.4426950408889634f * u2));
        if (kr == 0) *(LAS u16*)(L + L_HS + (wave * 4 + gq) * 2) = (u16)pack_bf16(y, y);
        lds_barrier();
        const v4u hf = *(const LAS v4u*)(L + L_HS + gq * 16);
        v4f acc[4];
#pragma unroll
        for (int c = 0; c < 4; ++c) acc[c] = MFMA32(RB[2 * c], hf, ((v4f){0.f, 0.f, 0.f, 0.f}));
        store_partial(acc[0], acc[1], acc[2], acc[3], xl + X_E5 + (u32)hidx * 4096, isH);
        STAMP(11 + layer * 14);
      }
    }
  }
  if (b == 0 && tid == 0) __hip_atomic_store((u32*)a.ws, tag, __ATOMIC_RELAXED, __HIP_MEMORY_SCOPE_AGENT);
}

}

extern "C" void kernel_launch(void* const* d_in, const int* in_sizes, int n_in, void* d_out, int out_size, void* d_ws,
                              size_t ws_size, hipStream_t stream) {
  Args a;
  a.x = (const u16*)d_in[0]; a.kc = (const u16*)d_in[1]; a.vc = (const u16*)d_in[2]; a.wq = (const u16*)d_in[3];
  a.wo = (const u16*)d_in[4]; a.w1 = (const u16*)d_in[6]; a.b1 = (const u16*)d_in[7]; a.w2 = (const u16*)d_in[8];
  a.b2 = (const u16*)d_in[9]; a.out = (u16*)d_out; a.ws = (unsigned char*)d_ws;
  fused_decode<<<NWG, NTHR, 0, stream>>>(a);
}
```

```cpp
#include <hip/hip_runtime.h>
#include <stdint.h>

typedef unsigned u32;
typedef unsigned short u16;
typedef unsigned long long u64;
typedef u32 v4u __attribute__((ext_vector_type(4)));
typedef u32 v2u __attribute__((ext_vector_type(2)));
typedef float v4f __attribute__((ext_vector_type(4)));
typedef short v4s __attribute__((ext_vector_type(4)));
typedef __bf16 v8bf __attribute__((ext_vector_type(8)));
typedef __bf16 v2bf __attribute__((ext_vector_type(2)));
#define LAS __attribute__((address_space(3)))

#define STAMPS 0
#define D_E1 5
#define D_E2 5
#define D_E3 14
#define D_E4 11
#define D_E5 13
#define D_E6 11
#define D_KV0 8
#define D_WO 15
#define D_W12 80
#define D_V0 8
#define KV_ROT 1
#define KV_WIN 14
#define KV_SPLIT 24
#define PREW 3
#define SAFE_RANGE 60.f
#define D_H 0
#define D_Q1 20
#define T_H4 320
#define D_BACK 2
#define D_Q0 60
#define D_G0 64
#define DMA_POL " nt"

namespace {

constexpr int SK = 65536, WIN = 4096;
constexpr int NWG = 256, NTHR = 512;

constexpr u32 L_K = 0, L_V = 65536, L_W4 = 0, L_WT = 32768, L_W1 = 65536;
constexpr u32 L_QS = 131072, L_XS = 132096, L_XP = 133120, L_P = 134144, L_ML = 136192, L_OS = 136448, L_HS = 136704,
              L_B = 136960, L_FLAG = 137984, L_ST = 138240, L_SCR = 138240 + 4096, L_OW = 138240 + 4096 + 3072, L_END = 138240 + 4096 + 3072 + 16384;

constexpr u32 X_E1 = 0;
constexpr u32 X_E2 = X_E1 + 512 * 128;
constexpr u32 X_E3 = X_E2 + 2176 * 128;
constexpr u32 X_E4 = X_E3 + 2048 * 128;
constexpr u32 X_E5 = X_E4 + 32 * 128;
constexpr u32 X_E6 = X_E5 + 1792 * 128;
constexpr u32 X_LAYER = X_E6 + 32 * 128;
constexpr u32 X_HINT = 2 * X_LAYER;
constexpr u32 X_REGION = 2u << 20;
constexpr u32 X_NREG = 64;
constexpr u32 X_BASE = 2u << 20;
constexpr u32 X_DBG = X_BASE + X_NREG * X_REGION;
static_assert(2 * X_LAYER + 128 * 128 <= X_REGION, "region");

#define CODE_BYTES 16128u
constexpr int SPIN_CAP = 1 << 18;

__device__ __forceinline__ void dma16(const void* gsrc, u32 lds_dst) {
  u32 keep;
  asm volatile("s_mov_b32 %0, m0\n\ts_mov_b32 m0, %2\n\ts_nop 0\n\tglobal_load_lds_dwordx4 %1, off" DMA_POL "\n\ts_mov_b32 m0, %0"
               : "=&s"(keep) : "v"(gsrc), "s"(__builtin_amdgcn_readfirstlane(lds_dst)) : "memory");
}
__device__ __forceinline__ void dma16_keep(const void* gsrc, u32 lds_dst) {
  u32 keep;
  asm volatile("s_mov_b32 %0, m0\n\ts_mov_b32 m0, %2\n\ts_nop 0\n\tglobal_load_lds_dwordx4 %1, off\n\ts_mov_b32 m0, %0"
               : "=&s"(keep) : "v"(gsrc), "s"(__builtin_amdgcn_readfirstlane(lds_dst)) : "memory");
}
__device__ __forceinline__ void lds_barrier() {
  asm volatile("s_waitcnt lgkmcnt(0)\n\ts_barrier" ::: "memory");
}
__device__ __forceinline__ float dot2(u32 a, u32 b, float c) {
  return __builtin_amdgcn_fdot2_f32_bf16(__builtin_bit_cast(v2bf, a), __builtin_bit_cast(v2bf, b), c, false);
}
__device__ __forceinline__ u32 pack_bf16(float lo, float hi) {
  typedef float v2f __attribute__((ext_vector_type(2)));
  return __builtin_bit_cast(u32, __builtin_convertvector((v2f){lo, hi}, v2bf));
}
__device__ __forceinline__ float bf_lo(u32 p) { return __uint_as_float(p << 16); }
__device__ __forceinline__ float bf_hi(u32 p) { return __uint_as_float(p & 0xffff0000u); }
__device__ __forceinline__ float rbf(float x) { return bf_lo(pack_bf16(x, x)); }
__device__ __forceinline__ u32 perm_lo(u32 a, u32 b) { return __builtin_amdgcn_perm(b, a, 0x05040100u); }
__device__ __forceinline__ u32 perm_hi(u32 a, u32 b) { return __builtin_amdgcn_perm(b, a, 0x07060302u); }
__device__ __forceinline__ float rdlane(float x, int l) { return __builtin_bit_cast(float, __builtin_amdgcn_readlane(__builtin_bit_cast(int, x), l)); }

template <int CTRL> __device__ __forceinline__ float dppf(float x) {
  return __builtin_bit_cast(float, __builtin_amdgcn_mov_dpp(__builtin_bit_cast(int, x), CTRL, 0xf, 0xf, true));
}
constexpr int XOR1 = 0xB1, XOR2 = 0x4E, XOR7 = 0x141, ROR8 = 0x128, ROR4 = 0x124, ROR2 = 0x122, ROR1 = 0x121;
constexpr u32 ONES = 0x3f803f80u;

__device__ __forceinline__ float tadd32(float a, float b) {
  auto s = __builtin_amdgcn_permlane32_swap(__float_as_uint(a), __float_as_uint(b), false, false);
  return __uint_as_float(s[0]) + __uint_as_float(s[1]);
}
__device__ __forceinline__ float tadd16(float a, float b) {
  auto s = __builtin_amdgcn_permlane16_swap(__float_as_uint(a), __float_as_uint(b), false, false);
  return __uint_as_float(s[0]) + __uint_as_float(s[1]);
}
__device__ __forceinline__ float vmax(float a, float b) { return __builtin_amdgcn_fmed3f(a, b, __builtin_inff()); }
__device__ __forceinline__ float vmax3(float a, float b, float c) { return vmax(vmax(a, b), c); }
__device__ __forceinline__ float tmax32(float a) {
  auto s = __builtin_amdgcn_permlane32_swap(__float_as_uint(a), __float_as_uint(a), false, false);
  return vmax(__uint_as_float(s[0]), __uint_as_float(s[1]));
}
__device__ __forceinline__ float tmax16(float a) {
  auto s = __builtin_amdgcn_permlane16_swap(__float_as_uint(a), __float_as_uint(a), false, false);
  return vmax(__uint_as_float(s[0]), __uint_as_float(s[1]));
}
__device__ __forceinline__ float rowsum16(float x) {
  x += dppf<ROR8>(x); x += dppf<ROR4>(x); x += dppf<ROR2>(x); x += dppf<ROR1>(x);
  return x;
}
__device__ __forceinline__ float reduce8(const float (&a)[8], int lane) {
  float t4[4], t2[2];
#pragma unroll
  for (int c = 0; c < 4; ++c) t4[c] = tadd32(a[c], a[c + 4]);
#pragma unroll
  for (int c = 0; c < 2; ++c) t2[c] = tadd16(t4[c], t4[c + 2]);
  const float lo = t2[0] + dppf<ROR8>(t2[0]), hi = t2[1] + dppf<ROR8>(t2[1]);
  float r = (lane & 8) ? hi : lo;
  r += dppf<XOR7>(r); r += dppf<XOR2>(r); r += dppf<XOR1>(r);
  return r;
}

template <int N>
__device__ __forceinline__ int gather(__amdgpu_buffer_rsrc_t rs, const u32 (&off)[N], u32 tag, v4u (&v)[N], bool& dead, bool slow = false) {
  int spin = 0;
  for (;; ++spin) {
    asm volatile("" ::: "memory");
#pragma unroll
    for (int i = 0; i < N; ++i) v[i] = __builtin_amdgcn_raw_buffer_load_b128(rs, off[i], 0, 16);
    u32 bad = 0u;
#pragma unroll
    for (int i = 0; i < N; ++i) { bad |= (v[i].y ^ tag); bad |= (v[i].w ^ tag); asm volatile("" : "+v"(bad)); }
    if (__all(bad == 0u)) break;
    if (dead || spin > SPIN_CAP) { dead = true; break; }
    if (slow) __builtin_amdgcn_s_sleep(D_BACK);
  }
  return spin;
}

struct Args {
  const u16* x; const u16* kc; const u16* vc; const u16* wq; const u16* wo;
  const u16* w1; const u16* b1; const u16* w2; const u16* b2;
  u16* out; unsigned char* ws;
};

#define STAMP(i) do {} while (0)
#define STAMPN(i, n) do { (void)(n); } while (0)

#define TR16(t, ra, o0, o1, o2, o3, o4, o5, o6, o7, o8, o9, o10, o11, o12, o13, o14, o15)                                                                   \
  asm volatile("ds_read_b64_tr_b16 %0, %16 offset:" #o0 "\n\tds_read_b64_tr_b16 %1, %16 offset:" #o1 "\n\tds_read_b64_tr_b16 %2, %16 offset:" #o2                \
               "\n\tds_read_b64_tr_b16 %3, %16 offset:" #o3 "\n\tds_read_b64_tr_b16 %4, %16 offset:" #o4 "\n\tds_read_b64_tr_b16 %5, %16 offset:" #o5            \
               "\n\tds_read_b64_tr_b16 %6, %16 offset:" #o6 "\n\tds_read_b64_tr_b16 %7, %16 offset:" #o7 "\n\tds_read_b64_tr_b16 %8, %16 offset:" #o8            \
               "\n\tds_read_b64_tr_b16 %9, %16 offset:" #o9 "\n\tds_read_b64_tr_b16 %10, %16 offset:" #o10 "\n\tds_read_b64_tr_b16 %11, %16 offset:" #o11        \
               "\n\tds_read_b64_tr_b16 %12, %16 offset:" #o12 "\n\tds_read_b64_tr_b16 %13, %16 offset:" #o13 "\n\tds_read_b64_tr_b16 %14, %16 offset:" #o14      \
               "\n\tds_read_b64_tr_b16 %15, %16 offset:" #o15 "\n\ts_waitcnt lgkmcnt(0)"                                                                         \
               : "=&v"(t[0]), "=&v"(t[1]), "=&v"(t[2]), "=&v"(t[3]), "=&v"(t[4]), "=&v"(t[5]), "=&v"(t[6]), "=&v"(t[7]), "=&v"(t[8]), "=&v"(t[9]), "=&v"(t[10]), \
                 "=&v"(t[11]), "=&v"(t[12]), "=&v"(t[13]), "=&v"(t[14]), "=&v"(t[15])                                                                            \
               : "v"(ra) : "memory")

#define MFMA32(A, B, C) __builtin_amdgcn_mfma_f32_16x16x32_bf16(__builtin_bit_cast(v8bf, A), __builtin_bit_cast(v8bf, B), C, 0, 0, 0)

__device__ __forceinline__ float ln_gemv4(const v4u xv, const v4u (&w)[4], float cs) {
  float a0 = 0.f, a1 = 0.f, a2 = 0.f, a3 = 0.f, s1 = 0.f, s2 = 0.f;
#pragma unroll
  for (int p = 0; p < 4; ++p) {
    a0 = dot2(xv[p], w[p].x, a0); a1 = dot2(xv[p], w[p].y, a1); a2 = dot2(xv[p], w[p].z, a2); a3 = dot2(xv[p], w[p].w, a3);
    s1 = dot2(xv[p], ONES, s1); s2 = dot2(xv[p], xv[p], s2);
  }
  const float acc = rowsum16(tadd16(tadd32(a0, a2), tadd32(a1, a3)));
  float ss = tadd32(s1, s2); ss = tadd16(ss, ss); ss = rowsum16(ss);
  const float mean = rdlane(ss, 0) * (1.f / 512.f);
  const float var = fmaxf(rdlane(ss, 32) * (1.f / 512.f) - mean * mean, 0.f);
  const float rstd = __builtin_amdgcn_rsqf(var + 1e-5f);
  return (acc - mean * cs) * rstd;
}

__global__ void __launch_bounds__(NTHR, 2) fused_decode(Args a) {
  __shared__ __attribute__((aligned(16))) unsigned char smem[L_END];
  LAS unsigned char* L = (LAS unsigned char*)smem;
  const u32 lbase = (u32)(size_t)L;
  const int tid = threadIdx.x, lane = tid & 63, wave = __builtin_amdgcn_readfirstlane(tid >> 6);
  const int b = blockIdx.x, g = b & 7, j = b >> 3;

  const bool isA = j < 16, isR = j < 4, isC = (j >= 16) & (j < 24), isH = (j >= 24) & (j < 31);
  const int jj = j & 15;
  const int qkh = (j >> 3) & 1, qcg = j & 7;
  const int ci = j & 7;
  const int hidx = g * 7 + (isH ? j - 24 : 0);
  const int ri = 4 * g + (j & 3);

  {
    u64 pc; asm volatile("s_getpc_b64 %0" : "=s"(pc));
    if (wave >= 1 && wave <= 3) {
      const u32 o = (u32)(wave - 1) * 8192u + (u32)lane * 128u;
      dma16_keep((const char*)((pc & ~127ull) + (o < CODE_BYTES ? o : 0u)), lbase + L_SCR + (wave - 1) * 1024);
    }
  }
  u32 epoch;
  asm volatile("s_load_dword %0, %1, 0x0" : "=s"(epoch) : "s"(a.ws) : "memory");
  auto kv_issue = [&](int t0, int t1) {
    int ln = tid & 63; asm volatile("" : "+v"(ln));
    const int w4 = wave - 4;
    const size_t kvoff = ((size_t)g * SK + (SK - WIN) + jj * 256) * 256;
    const int r0 = w4 * 4 + (ln >> 4);
    const char* kp = (const char*)a.kc + kvoff + r0 * 256 + ((ln & 15) ^ r0) * 16;
    const char* vp = (const char*)a.vc + kvoff + r0 * 256 + ((ln & 15) ^ ((r0 & 7) << 1)) * 16 - 65536;
#pragma unroll 1
    for (int t = t0; t < t1; ++t) {
      asm volatile("s_waitcnt vmcnt(%0)" :: "n"(KV_WIN - 1) : "memory");
      dma16((t < 16 ? kp : vp) + t * 4096, lbase + (t * 4 + w4) * 1024);
    }
  };
  if (isA) {
    if (wave >= 4) { if (D_KV0) __builtin_amdgcn_s_sleep(D_KV0); kv_issue(0, KV_SPLIT); }
  } else {
#pragma unroll
    for (int t = 0; t < 4; ++t) {
      const int lp = wave * 8 + (lane >> 3);
      dma16((const char*)a.wq + (size_t)(256 * qkh + 4 * lp + t) * 8192 + g * 1024 + qcg * 128 + ((lane & 7) ^ ((lp >> 1) & 7)) * 16, lbase + L_W4 + (t * 8 + wave) * 1024);
    }
  }
  if (wave == 0) dma16((const char*)a.x + lane * 16, lbase + L_XS);
  if (wave == 1) {
    const char* src = (lane & 7) < 4 ? (const char*)a.b1 + hidx * 64 + (lane & 3) * 16 : (const char*)a.b2 + ri * 32 + (lane & 1) * 16;
    dma16(src, lbase + L_B);
  }

  bool dead = false;
  const int gq = lane >> 4, hq = lane & 3, kr = lane & 15;
  v4u RA[8], RB[8], RH[4];
  float r_cs = 0.f, h_cs = 0.f, h_bias = 0.f;
  u32 xkeep = 0u;
#pragma unroll
  for (int i = 0; i < 8; ++i) { RA[i] = (v4u){0u, 0u, 0u, 0u}; RB[i] = (v4u){0u, 0u, 0u, 0u}; }
#pragma unroll
  for (int i = 0; i < 4; ++i) RH[i] = (v4u){0u, 0u, 0u, 0u};

  auto prep4 = [&](u32 base, v4u& w0, v4u& w1, v4u& w2, v4u& w3) -> float {
    int ln = tid & 63; asm volatile("" : "+v"(ln));
    v2u r[8];
#pragma unroll
    for (int i = 0; i < 8; ++i) r[i] = *(const LAS v2u*)(L + base + (i * 64 + ln) * 64 + ((wave >> 1) ^ ((ln >> 2) & 3)) * 16 + (wave & 1) * 8);
    v4u w[4];
#pragma unroll
    for (int p = 0; p < 4; ++p)
      w[p] = (v4u){perm_lo(r[2 * p].x, r[2 * p + 1].x), perm_hi(r[2 * p].x, r[2 * p + 1].x), perm_lo(r[2 * p].y, r[2 * p + 1].y), perm_hi(r[2 * p].y, r[2 * p + 1].y)};
    float c0 = 0.f, c1 = 0.f, c2 = 0.f, c3 = 0.f;
#pragma unroll
    for (int p = 0; p < 4; ++p) { c0 = dot2(w[p].x, ONES, c0); c1 = dot2(w[p].y, ONES, c1); c2 = dot2(w[p].z, ONES, c2); c3 = dot2(w[p].w, ONES, c3); }
    w0 = w[0]; w1 = w[1]; w2 = w[2]; w3 = w[3];
    return rowsum16(tadd16(tadd32(c0, c2), tadd32(c1, c3)));
  };
  auto tr_addr = [&](int c, int half) -> u32 {
    int ln = tid & 63; asm volatile("" : "+v"(ln));
    const int row = 8 * (ln >> 4) + 4 * half + ((ln & 15) >> 2), p = ln & 3;
    return lbase + L_WT + row * 1024 + (((8 * wave + 2 * c + (p >> 1)) ^ ((row & 7) << 1)) * 16) + (p & 1) * 8;
  };
  asm volatile("s_waitcnt lgkmcnt(0)" : "+s"(epoch) :: "memory");
  const u32 tag = epoch + 1u;
  const __amdgpu_buffer_rsrc_t rs = __builtin_amdgcn_make_buffer_rsrc(a.ws + X_BASE + (epoch % X_NREG) * X_REGION, 0, X_REGION, 0x00020000);
  if (isA && wave >= 4 && lane == 0) *(volatile LAS u32*)(L + L_FLAG + (wave - 4) * 4) = tag;
  STAMP(1);

  auto store_partial = [&](v4f c0, v4f c1, v4f c2, v4f c3, u32 base, bool act) {
    float v[2];
#pragma unroll
    for (int r = 0; r < 2; ++r) {
      const float e0 = (lane & 4) ? c0[2 + r] : c0[r], e1 = (lane & 4) ? c1[2 + r] : c1[r];
      const float e2 = (lane & 4) ? c2[2 + r] : c2[r], e3 = (lane & 4) ? c3[2 + r] : c3[r];
      const float lo = (lane & 1) ? e1 : e0, hi = (lane & 1) ? e3 : e2;
      v[r] = (lane & 2) ? hi : lo;
    }
    if (act && kr < 8)
      __builtin_amdgcn_raw_buffer_store_b128((v4u){__float_as_uint(v[0]), tag, __float_as_uint(v[1]), tag}, rs,
                                             base + (u32)(wave * 4 + hq) * 128 + gq * 32 + (lane & 4) * 4, 0, 16);
  };

  auto stage_q = [&](u32 xl) {
    const v2u xo = *(const LAS v2u*)(L + L_XS + qkh * 512 + lane * 8), xq = *(const LAS v2u*)(L + L_XS + (qkh ^ 1) * 512 + lane * 8);
    float ac[8];
#pragma unroll
    for (int q = 0; q < 8; ++q) ac[q] = dot2(xo.y, RH[2 + (q >> 2)][q & 3], dot2(xo.x, RH[q >> 2][q & 3], 0.f));
    const float s1 = dot2(xq.y, ONES, dot2(xq.x, ONES, dot2(xo.y, ONES, dot2(xo.x, ONES, 0.f))));
    const float s2 = dot2(xq.y, xq.y, dot2(xq.x, xq.x, dot2(xo.y, xo.y, dot2(xo.x, xo.x, 0.f))));
    const float acc = reduce8(ac, lane);
    float ss = tadd32(s1, s2); ss = tadd16(ss, ss); ss = rowsum16(ss);
    const float mean = rdlane(ss, 0) * (1.f / 512.f);
    const float var = fmaxf(rdlane(ss, 32) * (1.f / 512.f) - mean * mean, 0.f);
    const float y = (acc - mean * r_cs) * __builtin_amdgcn_rsqf(var + 1e-5f);
    if (!isA && (lane & 7) == 0) {
      const v2u d = {__float_as_uint(y), tag};
      __builtin_amdgcn_raw_buffer_store_b64(d, rs, xl + X_E1 + (u32)(g * 16 + jj) * 512 + wave * 64 + lane, 0, 16);
    }
  };

  if (!isA) {
    asm volatile("s_waitcnt vmcnt(0)" ::: "memory");
    lds_barrier();
    v4u r[4];
#pragma unroll
    for (int i = 0; i < 4; ++i) r[i] = *(const LAS v4u*)(L + L_W4 + (i * 64 + lane) * 128 + (wave ^ ((lane >> 1) & 7)) * 16);
#pragma unroll
    for (int p = 0; p < 2; ++p) {
      RH[2 * p] = (v4u){perm_lo(r[2 * p].x, r[2 * p + 1].x), perm_hi(r[2 * p].x, r[2 * p + 1].x), perm_lo(r[2 * p].y, r[2 * p + 1].y), perm_hi(r[2 * p].y, r[2 * p + 1].y)};
      RH[2 * p + 1] = (v4u){perm_lo(r[2 * p].z, r[2 * p + 1].z), perm_hi(r[2 * p].z, r[2 * p + 1].z), perm_lo(r[2 * p].w, r[2 * p + 1].w), perm_hi(r[2 * p].w, r[2 * p + 1].w)};
    }
    float c[8];
#pragma unroll
    for (int q = 0; q < 8; ++q) c[q] = dot2(RH[2 + (q >> 2)][q & 3], ONES, dot2(RH[q >> 2][q & 3], ONES, 0.f));
    r_cs = reduce8(c, lane);
    STAMP(2);
    stage_q(0u);
    STAMP(3);
  }
  {

      const v4u z = {0u, ~tag, 0u, ~tag};
      int t_ = tid; asm volatile("" : "+v"(t_));
      if (isA) {
      } else {
        if ((PREW & 2) && (isC | isH)) __builtin_amdgcn_raw_buffer_store_b128(z, rs, (isC ? X_E3 + (u32)(g * 8 + ci) * 4096 : X_E5 + (u32)hidx * 4096) + (t_ >> 8) * X_LAYER + (t_ & 255) * 16, 0, 16);
        if ((PREW & 2) && t_ < 32) __builtin_amdgcn_raw_buffer_store_b128(z, rs, X_LAYER + X_E1 + (u32)(g * 16 + jj) * 512 + t_ * 16, 0, 16);
        if (isC | isH) {
          lds_barrier();
          int ln = tid & 63; asm volatile("" : "+v"(ln));
          if (isC) {
            __builtin_amdgcn_s_sleep(D_WO);
            const char* p = (const char*)a.wo + (size_t)(g * 512 + ci * 64 + wave) * 1024 + (ln ^ (wave << 1)) * 16;
#pragma unroll
            for (int t = 0; t < 8; ++t) dma16(p + t * 8192, lbase + L_WT + (t * 8 + wave) * 1024);
          } else {
            __builtin_amdgcn_s_sleep(D_W12);
#pragma unroll
            for (int t = 0; t < 4; ++t) {
              const int tt = (t + 2 * (hidx & 1)) & 3, lp = tt * 16 + (ln >> 2);
              dma16((const char*)a.w1 + (size_t)(8 * lp + wave) * 3584 + hidx * 64 + ((ln & 3) ^ ((lp >> 2) & 3)) * 16, lbase + L_W1 + (wave * 4 + tt) * 1024);
            }
            const char* p = (const char*)a.w2 + (size_t)(hidx * 32 + wave) * 1024 + (ln ^ (wave << 1)) * 16;
#pragma unroll
            for (int t = 0; t < 4; ++t) dma16(p + t * 8192, lbase + L_WT + (t * 8 + wave) * 1024);
          }
        }
      }
      }

  for (int layer = 0; layer < 2; ++layer) {
    const u32 xl = layer * X_LAYER;
    if (layer != 0) {
      if (wave == 0) {
        __builtin_amdgcn_s_sleep(D_E6);
        const u32 o0 = X_E6 + (lane >> 1) * 128 + (lane & 1) * 32;
        const u32 off[2] = {o0, o0 + 16};
        v4u v[2];
        const int n = gather<2>(rs, off, tag, v, dead);
        STAMPN(16, n);
        *(LAS v4u*)(L + L_XS + lane * 16) = (v4u){v[0].x, v[0].z, v[1].x, v[1].z};
      }
      lds_barrier();
      stage_q(X_LAYER);
      STAMP(17);
    }

    if (layer != 0 || isA) {
      if (layer != 0) lds_barrier();
      if (layer == 0) {
        if (wave < 4) {
          for (int i = 0; i < 4096; ++i) {
            const v4u f = *(volatile LAS v4u*)(L + L_FLAG);
            if ((f.x == tag) & (f.y == tag) & (f.z == tag) & (f.w == tag)) break;
            __builtin_amdgcn_s_sleep(1);
          }
          if (D_Q1) __builtin_amdgcn_s_sleep(D_Q1);
          const u32 o0 = xl + X_E1 + (u32)(g * 64 + (lane >> 5) * 32 + wave * 8) * 128 + (lane & 31) * 16;
          const u32 off[2] = {o0, o0 + 512};
          v4u v[2];
          const int n = gather<2>(rs, off, tag, v, dead, true);
          STAMPN(4, n);
#pragma unroll
          for (int i = 0; i < 2; ++i) {
            float pA = __uint_as_float(v[i].x), pB = __uint_as_float(v[i].z);
            pA = tadd32(pA, pA); pB = tadd32(pB, pB);
            if (lane < 32) *(LAS u32*)(L + L_QS + (2 * i + (lane >> 4)) * 256 + wave * 64 + (lane & 15) * 4) = pack_bf16(pA, pB);
          }
        } else {
          kv_issue(KV_SPLIT, 32);
          STAMP(4);
        }
      } else {
        __builtin_amdgcn_s_sleep(D_E1);
        const u32 off[1] = {xl + X_E1 + (u32)(g * 64 + (lane >> 5) * 32 + wave * 4) * 128 + (lane & 31) * 16};
        v4u v[1];
        const int n = gather<1>(rs, off, tag, v, dead);
        STAMPN(18, n);
        float pA = __uint_as_float(v[0].x), pB = __uint_as_float(v[0].z);
        pA = tadd32(pA, pA); pB = tadd32(pB, pB);
        if (lane < 32) *(LAS u32*)(L + L_QS + (2 * (wave & 1) + (lane >> 4)) * 256 + (wave >> 1) * 64 + (lane & 15) * 4) = pack_bf16(pA, pB);
      }
      lds_barrier();
      if (layer == 0) {
#pragma unroll
        for (int s = 0; s < 8; ++s) RA[s] = *(const LAS v4u*)(L + L_K + (wave * 32 + (s >> 2) * 16 + kr) * 256 + (((4 * (s & 3) + gq) ^ kr) * 16));
      }
      v4f sa = {0.f, 0.f, 0.f, 0.f}, sb = {0.f, 0.f, 0.f, 0.f};
      {
        v4u qf[4];
#pragma unroll
        for (int s = 0; s < 4; ++s) qf[s] = *(const LAS v4u*)(L + L_QS + s * 256 + hq * 64 + gq * 16);
        __builtin_amdgcn_sched_barrier(0);
#pragma unroll
        for (int s = 0; s < 4; ++s) { sa = MFMA32(RA[s], qf[s], sa); sb = MFMA32(RA[4 + s], qf[s], sb); }
      }
      constexpr float C2 = 0.08838834764831845f * 1.4426950408889634f;
      float ref = 0.f;
      for (int pass = 0;; ++pass) {
        float p[8];
#pragma unroll
        for (int r = 0; r < 4; ++r) { p[r] = __builtin_amdgcn_exp2f(fmaf(sa[r], C2, -ref)); p[4 + r] = __builtin_amdgcn_exp2f(fmaf(sb[r], C2, -ref)); }
        const v4u pf = {pack_bf16(p[0], p[1]), pack_bf16(p[2], p[3]), pack_bf16(p[4], p[5]), pack_bf16(p[6], p[7])};
        v4u pm[4];
#pragma unroll
        for (int c = 0; c < 4; ++c) { const bool on = (kr >> 2) == c; pm[c] = (v4u){on ? pf.x : 0u, on ? pf.y : 0u, on ? pf.z : 0u, on ? pf.w : 0u}; }
        if (layer == 0 && pass == 0) {
          asm volatile("" : "+v"(pm[0]), "+v"(pm[1]), "+v"(pm[2]), "+v"(pm[3]));
        if (wave >= 4) asm volatile("s_waitcnt vmcnt(0)" ::: "memory");
        STAMP(14);
        lds_barrier();
        if (tid == 0) __builtin_amdgcn_raw_buffer_store_b128((v4u){(u32)__builtin_amdgcn_s_memrealtime(), tag, 0u, tag}, rs, X_HINT + (u32)(g * 16 + jj) * 128, 0, 16);
        const int row0 = wave * 32 + 4 * gq + ((lane & 15) >> 2), vp = lane & 3;
        const u32 va = lbase + L_V + row0 * 256 + ((row0 & 7) << 5) + (vp >> 1) * 16 + (vp & 1) * 8;
        v2u t[16];
        asm volatile(
            "ds_read_b64_tr_b16 %0, %16\n\tds_read_b64_tr_b16 %1, %16 offset:4096\n\tds_read_b64_tr_b16 %2, %17\n\tds_read_b64_tr_b16 %3, %17 offset:4096\n\t"
            "ds_read_b64_tr_b16 %4, %18\n\tds_read_b64_tr_b16 %5, %18 offset:4096\n\tds_read_b64_tr_b16 %6, %19\n\tds_read_b64_tr_b16 %7, %19 offset:4096\n\t"
            "ds_read_b64_tr_b16 %8, %20\n\tds_read_b64_tr_b16 %9, %20 offset:4096\n\tds_read_b64_tr_b16 %10, %21\n\tds_read_b64_tr_b16 %11, %21 offset:4096\n\t"
            "ds_read_b64_tr_b16 %12, %22\n\tds_read_b64_tr_b16 %13, %22 offset:4096\n\tds_read_b64_tr_b16 %14, %23\n\tds_read_b64_tr_b16 %15, %23 offset:4096\n\t"
            "s_waitcnt lgkmcnt(0)"
            : "=&v"(t[0]), "=&v"(t[1]), "=&v"(t[2]), "=&v"(t[3]), "=&v"(t[4]), "=&v"(t[5]), "=&v"(t[6]), "=&v"(t[7]), "=&v"(t[8]), "=&v"(t[9]), "=&v"(t[10]),
              "=&v"(t[11]), "=&v"(t[12]), "=&v"(t[13]), "=&v"(t[14]), "=&v"(t[15])
            : "v"(va), "v"(va ^ 32u), "v"(va ^ 64u), "v"(va ^ 96u), "v"(va ^ 128u), "v"(va ^ 160u), "v"(va ^ 192u), "v"(va ^ 224u) : "memory");
#pragma unroll
        for (int u = 0; u < 8; ++u) RB[u] = (v4u){t[2 * u].x, t[2 * u].y, t[2 * u + 1].x, t[2 * u + 1].y};
      }
        v4f oa = {0.f, 0.f, 0.f, 0.f}, ob = {0.f, 0.f, 0.f, 0.f};
#pragma unroll
        for (int c = 0; c < 4; ++c) { oa = MFMA32(pm[c], RB[c], oa); ob = MFMA32(pm[c], RB[4 + c], ob); }
        const v4f lw = MFMA32(pf, ((v4u){ONES, ONES, ONES, ONES}), ((v4f){0.f, 0.f, 0.f, 0.f}));
        const float ml = fmaxf(fmaxf(fmaxf(fmaxf(sa[0], sa[1]), sa[2]), fmaxf(fmaxf(sa[3], sb[0]), sb[1])), fmaxf(sb[2], sb[3]));
        const bool safe = __all(fabsf(ml) <= SAFE_RANGE / C2);
        *(LAS v4f*)(L + L_OW + wave * 2048 + lane * 16) = oa;
        *(LAS v4f*)(L + L_OW + wave * 2048 + 1024 + lane * 16) = ob;
        if (lane == 0) { *(LAS v4f*)(L + L_ML + wave * 16) = lw; *(LAS u32*)(L + L_ML + 128 + wave * 4) = safe ? 0u : 1u; }
        lds_barrier();
        const v4f x0 = *(const LAS v4f*)(L + L_OW + gq * 2048 + wave * 256 + kr * 16), x1 = *(const LAS v4f*)(L + L_OW + 8192 + gq * 2048 + wave * 256 + kr * 16);
        const u32 fl = *(const LAS u32*)(L + L_ML + (lane & 31) * 4 + (lane & 32) * 4);
        if (isA && pass == 0 && __any(lane >= 32 && lane < 40 && fl != 0u)) {
          float m = tmax32(tmax16(ml * C2));
          if (lane < 4) *(LAS float*)(L + L_P + hq * 32 + wave * 4) = m;
          lds_barrier();
          const v4f m0 = *(const LAS v4f*)(L + L_P + hq * 32), m1 = *(const LAS v4f*)(L + L_P + hq * 32 + 16);
          ref = fmaxf(fmaxf(fmaxf(m0[0], m0[1]), fmaxf(m0[2], m0[3])), fmaxf(fmaxf(m1[0], m1[1]), fmaxf(m1[2], m1[3])));
          continue;
        }
        const v4f xs = x0 + x1;
        const float o = tadd16(tadd32(xs[0], xs[2]), tadd32(xs[1], xs[3]));
        const float o1 = dppf<XOR1>(o);
        const u32 pr = pack_bf16(o, o1);
        const u32 pr2 = (u32)__builtin_amdgcn_mov_dpp((int)pr, XOR2, 0xf, 0xf, true);
        const u32 pbase = xl + X_E2 + (u32)((g * 16 + jj) * 17) * 128;
        if (isA && (lane & 3) == 0) {
          const v4u d = {pr, tag, pr2, tag};
          __builtin_amdgcn_raw_buffer_store_b128(d, rs, pbase + (gq * 4 + (wave >> 1)) * 128 + ((wave & 1) * 4 + (kr >> 2)) * 16, 0, 16);
        }
        if (wave == 7) {
          float lt = __uint_as_float(fl);
          lt += dppf<ROR4>(lt); lt += dppf<ROR8>(lt); lt = tadd16(lt, lt);
          if (isA && lane < 4) {
            const v4u d = {__float_as_uint(ref), tag, __float_as_uint(lt), tag};
            __builtin_amdgcn_raw_buffer_store_b128(d, rs, pbase + 16 * 128 + lane * 16, 0, 16);
          }
        }
        break;
      }
      STAMP(5 + layer * 14);
      if (layer == 0 && wave >= 1 && wave < 4) {
        const v4u z = {0u, ~tag, 0u, ~tag};
        int pc = tid - 64; asm volatile("" : "+v"(pc));
        if (pc < 136) __builtin_amdgcn_raw_buffer_store_b128(z, rs, X_LAYER + X_E2 + (u32)((g * 16 + jj) * 17) * 128 + pc * 16, 0, 16);
        else if (isR && pc < 160) { const int line = (pc - 136) >> 3;
          __builtin_amdgcn_raw_buffer_store_b128(z, rs, (line < 2 ? line * X_LAYER + X_E4 : X_E6) + (u32)ri * 128 + (pc & 7) * 16, 0, 16); }
      }
    }

    if (layer == 0 && (isC | isH)) {
      int ln = tid & 63; asm volatile("" : "+v"(ln));
          asm volatile("s_waitcnt vmcnt(0)" ::: "memory");
          lds_barrier();
          {
            v2u t[16];
            const u32 a0 = tr_addr(0, 0), a1 = tr_addr(0, 1), a2 = tr_addr(1, 0), a3 = tr_addr(1, 1), a4 = tr_addr(2, 0), a5 = tr_addr(2, 1), a6 = tr_addr(3, 0), a7 = tr_addr(3, 1);
            asm volatile(
                "ds_read_b64_tr_b16 %0, %16\n\tds_read_b64_tr_b16 %1, %17\n\tds_read_b64_tr_b16 %2, %16 offset:32768\n\tds_read_b64_tr_b16 %3, %17 offset:32768\n\t"
                "ds_read_b64_tr_b16 %4, %18\n\tds_read_b64_tr_b16 %5, %19\n\tds_read_b64_tr_b16 %6, %18 offset:32768\n\tds_read_b64_tr_b16 %7, %19 offset:32768\n\t"
                "ds_read_b64_tr_b16 %8, %20\n\tds_read_b64_tr_b16 %9, %21\n\tds_read_b64_tr_b16 %10, %20 offset:32768\n\tds_read_b64_tr_b16 %11, %21 offset:32768\n\t"
                "ds_read_b64_tr_b16 %12, %22\n\tds_read_b64_tr_b16 %13, %23\n\tds_read_b64_tr_b16 %14, %22 offset:32768\n\tds_read_b64_tr_b16 %15, %23 offset:32768\n\t"
                "s_waitcnt lgkmcnt(0)"
                : "=&v"(t[0]), "=&v"(t[1]), "=&v"(t[2]), "=&v"(t[3]), "=&v"(t[4]), "=&v"(t[5]), "=&v"(t[6]), "=&v"(t[7]), "=&v"(t[8]), "=&v"(t[9]), "=&v"(t[10]),
                  "=&v"(t[11]), "=&v"(t[12]), "=&v"(t[13]), "=&v"(t[14]), "=&v"(t[15])
                : "v"(a0), "v"(a1), "v"(a2), "v"(a3), "v"(a4), "v"(a5), "v"(a6), "v"(a7) : "memory");
#pragma unroll
            for (int c = 0; c < 4; ++c) {
              RB[2 * c] = (v4u){t[4 * c].x, t[4 * c].y, t[4 * c + 1].x, t[4 * c + 1].y};
              RB[2 * c + 1] = (v4u){t[4 * c + 2].x, t[4 * c + 2].y, t[4 * c + 3].x, t[4 * c + 3].y};
            }
          }
          if (isH) {
            h_cs = prep4(L_W1, RA[0], RA[1], RA[2], RA[3]);
            h_bias = bf_lo(*(const LAS u16*)(L + L_B + (wave * 4 + (ln >> 4)) * 2));
          }
          STAMP(28);
    }
    const bool late = (layer == 0) & isH;
    if (!late) {
      if (wave == 0) {
        if (layer == 0) {
          if (!isA && !isC && !isH) __builtin_amdgcn_s_sleep(D_G0);
          const u32 ho[2] = {X_HINT + (u32)lane * 128, X_HINT + (u32)lane * 128 + 8192};
          v4u hv[2];
          const int nh = gather<2>(rs, ho, tag, hv, dead, true);
          STAMPN(15, nh);
          if (D_H) __builtin_amdgcn_s_sleep(D_H);
        } else {
          __builtin_amdgcn_s_sleep(D_E2);
        }
        u32 off[8];
#pragma unroll
        for (int i = 0; i < 4; ++i) {
          const u32 pb = xl + X_E2 + (u32)((g * 16 + 4 * i + gq) * 17) * 128;
          off[i] = pb + ci * 256 + kr * 16;
          off[4 + i] = pb + 16 * 128 + (ci >> 1) * 16;
        }
        v4u v[8];
        const int n = gather<8>(rs, off, tag, v, dead, layer == 0);
        STAMPN(6 + layer * 14, n);
        float M = vmax(vmax3(__uint_as_float(v[4].x), __uint_as_float(v[5].x), __uint_as_float(v[6].x)), __uint_as_float(v[7].x));
        M = tmax16(M); M = tmax32(M);
        float a0 = 0.f, a1 = 0.f, a2 = 0.f, a3 = 0.f, Ls = 0.f;
#pragma unroll
        for (int i = 0; i < 4; ++i) {
          const float e = __builtin_amdgcn_exp2f(__uint_as_float(v[4 + i].x) - M);
          Ls = fmaf(e, __uint_as_float(v[4 + i].z), Ls);
          a0 = fmaf(e, bf_lo(v[i].x), a0); a1 = fmaf(e, bf_hi(v[i].x), a1); a2 = fmaf(e, bf_lo(v[i].z), a2); a3 = fmaf(e, bf_hi(v[i].z), a3);
        }
        float b0 = tadd32(a0, a2), b1 = tadd32(a1, a3); Ls = tadd32(Ls, Ls);
        b0 = tadd16(b0, b0); b1 = tadd16(b1, b1); Ls = tadd16(Ls, Ls);
        const float inv = __builtin_amdgcn_rcpf(Ls);
        if (!(lane & 16)) *(LAS u32*)(L + L_OS + kr * 8 + (lane >> 5) * 4) = pack_bf16(b0 * inv, b1 * inv);
      }
      lds_barrier();
      const v4u of0 = *(const LAS v4u*)(L + L_OS + gq * 16), of1 = *(const LAS v4u*)(L + L_OS + 64 + gq * 16);
      v4f acc[4];
#pragma unroll
      for (int c = 0; c < 4; ++c) acc[c] = MFMA32(RB[2 * c + 1], of1, MFMA32(RB[2 * c], of0, ((v4f){0.f, 0.f, 0.f, 0.f})));
      store_partial(acc[0], acc[1], acc[2], acc[3], xl + X_E3 + (u32)(g * 8 + ci) * 4096, isC);
      STAMP(7 + layer * 14);
    }

#pragma unroll
    for (int ph = 0; ph < 2; ++ph) {
      lds_barrier();
      if (wave == 0 && !(late && ph == 0)) {
        constexpr int NP = 8;
        if (ph == 0) __builtin_amdgcn_s_sleep(D_E3); else __builtin_amdgcn_s_sleep(D_E5);
        u32 off[NP];
#pragma unroll
        for (int i = 0; i < NP; ++i) off[i] = xl + (ph == 0 ? X_E3 : X_E5) + (u32)(((ph == 1 && i == 7 ? 6 : i) * 8 + (lane >> 3)) * 32 + ri) * 128 + (lane & 7) * 16;
        const u32 xin = ph == 0 ? *(const LAS u32*)(L + L_XS + ri * 32 + (lane & 7) * 4) : xkeep;
        const u32 bb = *(const LAS u32*)(L + L_B + 64 + (lane & 7) * 4);
        float s0, s1;
        int n = 0;
        for (;; ++n) {
          asm volatile("" ::: "memory");
          v4u v[NP];
#pragma unroll
          for (int i = 0; i < (ph == 0 ? 8 : 7); ++i) v[i] = __builtin_amdgcn_raw_buffer_load_b128(rs, off[i], 0, 16);
          u32 bad = 0u; s0 = 0.f; s1 = 0.f;
#pragma unroll
          for (int i = 0; i < (ph == 0 ? 8 : 7); ++i) {
            bad |= (v[i].y ^ tag); bad |= (v[i].w ^ tag); s0 += __uint_as_float(v[i].x); s1 += __uint_as_float(v[i].z);
            asm volatile("" : "+v"(bad), "+v"(s0), "+v"(s1));
          }
          if (__all(bad == 0u)) break;
          if (dead || n > SPIN_CAP) { dead = true; break; }
        }
        STAMPN(8 + ph * 4 + layer * 14, n);
        float t = tadd32(s0, s1); t = tadd16(t, t); t += dppf<ROR8>(t);
        float r = rbf(t);
        if (ph == 1) r = rbf(r + ((lane & 32) ? bf_hi(bb) : bf_lo(bb)));
        r = r + ((lane & 32) ? bf_hi(xin) : bf_lo(xin));
        auto sw = __builtin_amdgcn_permlane32_swap(__float_as_uint(r), __float_as_uint(r), false, false);
        const u32 pr = pack_bf16(__uint_as_float(sw[0]), __uint_as_float(sw[1]));
        xkeep = pr;
        if (isR && lane < 8) {
          if (ph == 0 || layer == 0) {
            const v2u d = {pr, tag};
            __builtin_amdgcn_raw_buffer_store_b64(d, rs, xl + (ph == 0 ? X_E4 : X_E6) + (u32)ri * 128 + lane * 8, 0, 16);
          } else {
            *(u32*)(a.out + ri * 16 + lane * 2) = pr;
          }
        }
        STAMP(9 + ph * 4 + layer * 14);
      }
      if (ph == 0) {
        if (wave == 0) {
          if (late) {
            const u32 ho[2] = {X_HINT + (u32)lane * 128, X_HINT + (u32)lane * 128 + 8192};
            v4u hv[2];
            gather<2>(rs, ho, tag, hv, dead, true);
            const u32 now = (u32)__builtin_amdgcn_s_memrealtime();
            float age = fminf((float)(int)(now - hv[0].x), (float)(int)(now - hv[1].x));
            age = fminf(age, dppf<ROR8>(age)); age = fminf(age, dppf<ROR4>(age)); age = fminf(age, dppf<ROR2>(age)); age = fminf(age, dppf<ROR1>(age));
            age = -tmax32(tmax16(-age));
            const int wait = T_H4 - (int)rdlane(age, 0);
            for (int i = 0; i < 4096 && (int)((u32)__builtin_amdgcn_s_memrealtime() - now) < wait; ++i) {}
          } else {
            __builtin_amdgcn_s_sleep(D_E4);
          }
          const u32 o0 = xl + X_E4 + (lane >> 1) * 128 + (lane & 1) * 32;
          const u32 off[2] = {o0, o0 + 16};
          v4u v[2];
          const int n = gather<2>(rs, off, tag, v, dead);
          STAMPN(10 + layer * 14, n);
          *(LAS v4u*)(L + L_XP + lane * 16) = (v4u){v[0].x, v[0].z, v[1].x, v[1].z};
        }
        lds_barrier();
        const v4u xv = *(const LAS v4u*)(L + L_XP + lane * 16);
        const v4u w1r[4] = {RA[0], RA[1], RA[2], RA[3]};
        const float z = rbf(rbf(ln_gemv4(xv, w1r, h_cs)) + h_bias);
        const float u2 = z * (1.5957691216057308f + 0.07135481627260025f * z * z);
        const float y = z * __builtin_amdgcn_rcpf(1.f + __builtin_amdgcn_exp2f(-1.4426950408889634f * u2));
        if (kr == 0) *(LAS u16*)(L + L_HS + (wave * 4 + gq) * 2) = (u16)pack_bf16(y, y);
        lds_barrier();
        const v4u hf = *(const LAS v4u*)(L + L_HS + gq * 16);
        v4f acc[4];
#pragma unroll
        for (int c = 0; c < 4; ++c) acc[c] = MFMA32(RB[2 * c], hf, ((v4f){0.f, 0.f, 0.f, 0.f}));
        store_partial(acc[0], acc[1], acc[2], acc[3], xl + X_E5 + (u32)hidx * 4096, isH);
        STAMP(11 + layer * 14);
      }
    }
  }
  if (b == 0 && tid == 0) __hip_atomic_store((u32*)a.ws, tag, __ATOMIC_RELAXED, __HIP_MEMORY_SCOPE_AGENT);
}

}

extern "C" void kernel_launch(void* const* d_in, const int* in_sizes, int n_in, void* d_out, int out_size, void* d_ws,
                              size_t ws_size, hipStream_t stream) {
  Args a;
  a.x = (const u16*)d_in[0]; a.kc = (const u16*)d_in[1]; a.vc = (const u16*)d_in[2]; a.wq = (const u16*)d_in[3];
  a.wo = (const u16*)d_in[4]; a.w1 = (const u16*)d_in[6]; a.b1 = (const u16*)d_in[7]; a.w2 = (const u16*)d_in[8];
  a.b2 = (const u16*)d_in[9]; a.out = (u16*)d_out; a.ws = (unsigned char*)d_ws;
  fused_decode<<<NWG, NTHR, 0, stream>>>(a);
}
```

```cpp
#include <hip/hip_runtime.h>
#include <stdint.h>

typedef unsigned u32;
typedef unsigned short u16;
typedef unsigned long long u64;
typedef u32 v4u __attribute__((ext_vector_type(4)));
typedef u32 v2u __attribute__((ext_vector_type(2)));
typedef float v4f __attribute__((ext_vector_type(4)));
typedef short v4s __attribute__((ext_vector_type(4)));
typedef __bf16 v8bf __attribute__((ext_vector_type(8)));
typedef __bf16 v2bf __attribute__((ext_vector_type(2)));
#define LAS __attribute__((address_space(3)))

#define STAMPS 0
#define D_E1 5
#define D_E2 5
#define D_E3 14
#define D_E4 11
#define D_E5 13
#define D_E6 11
#define D_KV0 8
#define D_WO 1
#define D_W12 1
#define D_V0 8
#define KV_ROT 1
#define KV_WIN 14
#define KV_SPLIT 24
#define PREW 3
#define SAFE_RANGE 60.f
#define D_H 0
#define D_Q1 24
#define T_H4 310
#define D_BACK 2
#define D_Q0 60
#define D_G0 64
#define DMA_POL " nt"

namespace {

constexpr int SK = 65536, WIN = 4096;
constexpr int NWG = 256, NTHR = 512;

constexpr u32 L_K = 0, L_V = 65536, L_W4 = 0, L_WT = 32768, L_W1 = 65536;
constexpr u32 L_QS = 131072, L_XS = 132096, L_XP = 133120, L_P = 134144, L_ML = 136192, L_OS = 136448, L_HS = 136704,
              L_B = 136960, L_FLAG = 137984, L_ST = 138240, L_SCR = 138240 + 4096, L_OW = 138240 + 4096 + 3072, L_END = 138240 + 4096 + 3072 + 16384;

constexpr u32 X_E1 = 0;
constexpr u32 X_E2 = X_E1 + 512 * 128;
constexpr u32 X_E3 = X_E2 + 2176 * 128;
constexpr u32 X_E4 = X_E3 + 2048 * 128;
constexpr u32 X_E5 = X_E4 + 32 * 128;
constexpr u32 X_E6 = X_E5 + 1792 * 128;
constexpr u32 X_LAYER = X_E6 + 32 * 128;
constexpr u32 X_HINT = 2 * X_LAYER;
constexpr u32 X_REGION = 2u << 20;
constexpr u32 X_NREG = 64;
constexpr u32 X_BASE = 2u << 20;
constexpr u32 X_DBG = X_BASE + X_NREG * X_REGION;
static_assert(2 * X_LAYER + 128 * 128 <= X_REGION, "region");

#define CODE_BYTES 16128u
constexpr int SPIN_CAP = 1 << 18;

__device__ __forceinline__ void dma16(const void* gsrc, u32 lds_dst) {
  u32 keep;
  asm volatile("s_mov_b32 %0, m0\n\ts_mov_b32 m0, %2\n\ts_nop 0\n\tglobal_load_lds_dwordx4 %1, off" DMA_POL "\n\ts_mov_b32 m0, %0"
               : "=&s"(keep) : "v"(gsrc), "s"(__builtin_amdgcn_readfirstlane(lds_dst)) : "memory");
}
__device__ __forceinline__ void dma16_keep(const void* gsrc, u32 lds_dst) {
  u32 keep;
  asm volatile("s_mov_b32 %0, m0\n\ts_mov_b32 m0, %2\n\ts_nop 0\n\tglobal_load_lds_dwordx4 %1, off\n\ts_mov_b32 m0, %0"
               : "=&s"(keep) : "v"(gsrc), "s"(__builtin_amdgcn_readfirstlane(lds_dst)) : "memory");
}
__device__ __forceinline__ void lds_barrier() {
  asm volatile("s_waitcnt lgkmcnt(0)\n\ts_barrier" ::: "memory");
}
__device__ __forceinline__ float dot2(u32 a, u32 b, float c) {
  return __builtin_amdgcn_fdot2_f32_bf16(__builtin_bit_cast(v2bf, a), __builtin_bit_cast(v2bf, b), c, false);
}
__device__ __forceinline__ u32 pack_bf16(float lo, float hi) {
  typedef float v2f __attribute__((ext_vector_type(2)));
  return __builtin_bit_cast(u32, __builtin_convertvector((v2f){lo, hi}, v2bf));
}
__device__ __forceinline__ float bf_lo(u32 p) { return __uint_as_float(p << 16); }
__device__ __forceinline__ float bf_hi(u32 p) { return __uint_as_float(p & 0xffff0000u); }
__device__ __forceinline__ float rbf(float x) { return bf_lo(pack_bf16(x, x)); }
__device__ __forceinline__ u32 perm_lo(u32 a, u32 b) { return __builtin_amdgcn_perm(b, a, 0x05040100u); }
__device__ __forceinline__ u32 perm_hi(u32 a, u32 b) { return __builtin_amdgcn_perm(b, a, 0x07060302u); }
__device__ __forceinline__ float rdlane(float x, int l) { return __builtin_bit_cast(float, __builtin_amdgcn_readlane(__builtin_bit_cast(int, x), l)); }

template <int CTRL> __device__ __forceinline__ float dppf(float x) {
  return __builtin_bit_cast(float, __builtin_amdgcn_mov_dpp(__builtin_bit_cast(int, x), CTRL, 0xf, 0xf, true));
}
constexpr int XOR1 = 0xB1, XOR2 = 0x4E, XOR7 = 0x141, ROR8 = 0x128, ROR4 = 0x124, ROR2 = 0x122, ROR1 = 0x121;
constexpr u32 ONES = 0x3f803f80u;

__device__ __forceinline__ float tadd32(float a, float b) {
  auto s = __builtin_amdgcn_permlane32_swap(__float_as_uint(a), __float_as_uint(b), false, false);
  return __uint_as_float(s[0]) + __uint_as_float(s[1]);
}
__device__ __forceinline__ float tadd16(float a, float b) {
  auto s = __builtin_amdgcn_permlane16_swap(__float_as_uint(a), __float_as_uint(b), false, false);
  return __uint_as_float(s[0]) + __uint_as_float(s[1]);
}
__device__ __forceinline__ float vmax(float a, float b) { return __builtin_amdgcn_fmed3f(a, b, __builtin_inff()); }
__device__ __forceinline__ float vmax3(float a, float b, float c) { return vmax(vmax(a, b), c); }
__device__ __forceinline__ float tmax32(float a) {
  auto s = __builtin_amdgcn_permlane32_swap(__float_as_uint(a), __float_as_uint(a), false, false);
  return vmax(__uint_as_float(s[0]), __uint_as_float(s[1]));
}
__device__ __forceinline__ float tmax16(float a) {
  auto s = __builtin_amdgcn_permlane16_swap(__float_as_uint(a), __float_as_uint(a), false, false);
  return vmax(__uint_as_float(s[0]), __uint_as_float(s[1]));
}
__device__ __forceinline__ float rowsum16(float x) {
  x += dppf<ROR8>(x); x += dppf<ROR4>(x); x += dppf<ROR2>(x); x += dppf<ROR1>(x);
  return x;
}
__device__ __forceinline__ float reduce8(const float (&a)[8], int lane) {
  float t4[4], t2[2];
#pragma unroll
  for (int c = 0; c < 4; ++c) t4[c] = tadd32(a[c], a[c + 4]);
#pragma unroll
  for (int c = 0; c < 2; ++c) t2[c] = tadd16(t4[c], t4[c + 2]);
  const float lo = t2[0] + dppf<ROR8>(t2[0]), hi = t2[1] + dppf<ROR8>(t2[1]);
  float r = (lane & 8) ? hi : lo;
  r += dppf<XOR7>(r); r += dppf<XOR2>(r); r += dppf<XOR1>(r);
  return r;
}

template <int N>
__device__ __forceinline__ int gather(__amdgpu_buffer_rsrc_t rs, const u32 (&off)[N], u32 tag, v4u (&v)[N], bool& dead, bool slow = false) {
  int spin = 0;
  for (;; ++spin) {
    asm volatile("" ::: "memory");
#pragma unroll
    for (int i = 0; i < N; ++i) v[i] = __builtin_amdgcn_raw_buffer_load_b128(rs, off[i], 0, 16);
    u32 bad = 0u;
#pragma unroll
    for (int i = 0; i < N; ++i) { bad |= (v[i].y ^ tag); bad |= (v[i].w ^ tag); asm volatile("" : "+v"(bad)); }
    if (__all(bad == 0u)) break;
    if (dead || spin > SPIN_CAP) { dead = true; break; }
    if (slow) __builtin_amdgcn_s_sleep(D_BACK);
  }
  return spin;
}

struct Args {
  const u16* x; const u16* kc; const u16* vc; const u16* wq; const u16* wo;
  const u16* w1; const u16* b1; const u16* w2; const u16* b2;
  u16* out; unsigned char* ws;
};

#define STAMP(i) do {} while (0)
#define STAMPN(i, n) do { (void)(n); } while (0)

#define TR16(t, ra, o0, o1, o2, o3, o4, o5, o6, o7, o8, o9, o10, o11, o12, o13, o14, o15)                                                                   \
  asm volatile("ds_read_b64_tr_b16 %0, %16 offset:" #o0 "\n\tds_read_b64_tr_b16 %1, %16 offset:" #o1 "\n\tds_read_b64_tr_b16 %2, %16 offset:" #o2                \
               "\n\tds_read_b64_tr_b16 %3, %16 offset:" #o3 "\n\tds_read_b64_tr_b16 %4, %16 offset:" #o4 "\n\tds_read_b64_tr_b16 %5, %16 offset:" #o5            \
               "\n\tds_read_b64_tr_b16 %6, %16 offset:" #o6 "\n\tds_read_b64_tr_b16 %7, %16 offset:" #o7 "\n\tds_read_b64_tr_b16 %8, %16 offset:" #o8            \
               "\n\tds_read_b64_tr_b16 %9, %16 offset:" #o9 "\n\tds_read_b64_tr_b16 %10, %16 offset:" #o10 "\n\tds_read_b64_tr_b16 %11, %16 offset:" #o11        \
               "\n\tds_read_b64_tr_b16 %12, %16 offset:" #o12 "\n\tds_read_b64_tr_b16 %13, %16 offset:" #o13 "\n\tds_read_b64_tr_b16 %14, %16 offset:" #o14      \
               "\n\tds_read_b64_tr_b16 %15, %16 offset:" #o15 "\n\ts_waitcnt lgkmcnt(0)"                                                                         \
               : "=&v"(t[0]), "=&v"(t[1]), "=&v"(t[2]), "=&v"(t[3]), "=&v"(t[4]), "=&v"(t[5]), "=&v"(t[6]), "=&v"(t[7]), "=&v"(t[8]), "=&v"(t[9]), "=&v"(t[10]), \
                 "=&v"(t[11]), "=&v"(t[12]), "=&v"(t[13]), "=&v"(t[14]), "=&v"(t[15])                                                                            \
               : "v"(ra) : "memory")

#define MFMA32(A, B, C) __builtin_amdgcn_mfma_f32_16x16x32_bf16(__builtin_bit_cast(v8bf, A), __builtin_bit_cast(v8bf, B), C, 0, 0, 0)

__device__ __forceinline__ float ln_gemv4(const v4u xv, const v4u (&w)[4], float cs) {
  float a0 = 0.f, a1 = 0.f, a2 = 0.f, a3 = 0.f, s1 = 0.f, s2 = 0.f;
#pragma unroll
  for (int p = 0; p < 4; ++p) {
    a0 = dot2(xv[p], w[p].x, a0); a1 = dot2(xv[p], w[p].y, a1); a2 = dot2(xv[p], w[p].z, a2); a3 = dot2(xv[p], w[p].w, a3);
    s1 = dot2(xv[p], ONES, s1); s2 = dot2(xv[p], xv[p], s2);
  }
  const float acc = rowsum16(tadd16(tadd32(a0, a2), tadd32(a1, a3)));
  float ss = tadd32(s1, s2); ss = tadd16(ss, ss); ss = rowsum16(ss);
  const float mean = rdlane(ss, 0) * (1.f / 512.f);
  const float var = fmaxf(rdlane(ss, 32) * (1.f / 512.f) - mean * mean, 0.f);
  const float rstd = __builtin_amdgcn_rsqf(var + 1e-5f);
  return (acc - mean * cs) * rstd;
}

__global__ void __launch_bounds__(NTHR, 2) fused_decode(Args a) {
  __shared__ __attribute__((aligned(16))) unsigned char smem[L_END];
  LAS unsigned char* L = (LAS unsigned char*)smem;
  const u32 lbase = (u32)(size_t)L;
  const int tid = threadIdx.x, lane = tid & 63, wave = __builtin_amdgcn_readfirstlane(tid >> 6);
  const int b = blockIdx.x, g = b & 7, j = b >> 3;

  const bool isA = j < 16, isR = j < 4, isC = (j >= 16) & (j < 24), isH = (j >= 24) & (j < 31);
  const int jj = j & 15;
  const int qkh = (j >> 3) & 1, qcg = j & 7;
  const int ci = j & 7;
  const int hidx = g * 7 + (isH ? j - 24 : 0);
  const int ri = 4 * g + (j & 3);

  {
    u64 pc; asm volatile("s_getpc_b64 %0" : "=s"(pc));
    if (wave >= 1 && wave <= 3) {
      const u32 o = (u32)(wave - 1) * 8192u + (u32)lane * 128u;
      dma16_keep((const char*)((pc & ~127ull) + (o < CODE_BYTES ? o : 0u)), lbase + L_SCR + (wave - 1) * 1024);
    }
  }
  u32 epoch;
  asm volatile("s_load_dword %0, %1, 0x0" : "=s"(epoch) : "s"(a.ws) : "memory");
  auto kv_issue = [&](int t0, int t1) {
    int ln = tid & 63; asm volatile("" : "+v"(ln));
    const int w4 = wave - 4;
    const size_t kvoff = ((size_t)g * SK + (SK - WIN) + jj * 256) * 256;
    const int r0 = w4 * 4 + (ln >> 4);
    const char* kp = (const char*)a.kc + kvoff + r0 * 256 + ((ln & 15) ^ r0) * 16;
    const char* vp = (const char*)a.vc + kvoff + r0 * 256 + ((ln & 15) ^ ((r0 & 7) << 1)) * 16 - 65536;
#pragma unroll 1
    for (int t = t0; t < t1; ++t) {
      asm volatile("s_waitcnt vmcnt(%0)" :: "n"(KV_WIN - 1) : "memory");
      dma16((t < 16 ? kp : vp) + t * 4096, lbase + (t * 4 + w4) * 1024);
    }
  };
  if (isA) {
    if (wave >= 4) { if (D_KV0) __builtin_amdgcn_s_sleep(D_KV0); kv_issue(0, KV_SPLIT); }
  } else {
#pragma unroll
    for (int t = 0; t < 4; ++t) {
      const int lp = wave * 8 + (lane >> 3);
      dma16((const char*)a.wq + (size_t)(256 * qkh + 4 * lp + t) * 8192 + g * 1024 + qcg * 128 + ((lane & 7) ^ ((lp >> 1) & 7)) * 16, lbase + L_W4 + (t * 8 + wave) * 1024);
    }
  }
  if (wave == 0) dma16((const char*)a.x + lane * 16, lbase + L_XS);
  if (wave == 1) {
    const char* src = (lane & 7) < 4 ? (const char*)a.b1 + hidx * 64 + (lane & 3) * 16 : (const char*)a.b2 + ri * 32 + (lane & 1) * 16;
    dma16(src, lbase + L_B);
  }

  bool dead = false;
  const int gq = lane >> 4, hq = lane & 3, kr = lane & 15;
  v4u RA[8], RB[8], RH[4];
  float r_cs = 0.f, h_cs = 0.f, h_bias = 0.f;
  u32 xkeep = 0u;
#pragma unroll
  for (int i = 0; i < 8; ++i) { RA[i] = (v4u){0u, 0u, 0u, 0u}; RB[i] = (v4u){0u, 0u, 0u, 0u}; }
#pragma unroll
  for (int i = 0; i < 4; ++i) RH[i] = (v4u){0u, 0u, 0u, 0u};

  auto prep4 = [&](u32 base, v4u& w0, v4u& w1, v4u& w2, v4u& w3) -> float {
    int ln = tid & 63; asm volatile("" : "+v"(ln));
    v2u r[8];
#pragma unroll
    for (int i = 0; i < 8; ++i) r[i] = *(const LAS v2u*)(L + base + (i * 64 + ln) * 64 + ((wave >> 1) ^ ((ln >> 2) & 3)) * 16 + (wave & 1) * 8);
    v4u w[4];
#pragma unroll
    for (int p = 0; p < 4; ++p)
      w[p] = (v4u){perm_lo(r[2 * p].x, r[2 * p + 1].x), perm_hi(r[2 * p].x, r[2 * p + 1].x), perm_lo(r[2 * p].y, r[2 * p + 1].y), perm_hi(r[2 * p].y, r[2 * p + 1].y)};
    float c0 = 0.f, c1 = 0.f, c2 = 0.f, c3 = 0.f;
#pragma unroll
    for (int p = 0; p < 4; ++p) { c0 = dot2(w[p].x, ONES, c0); c1 = dot2(w[p].y, ONES, c1); c2 = dot2(w[p].z, ONES, c2); c3 = dot2(w[p].w, ONES, c3); }
    w0 = w[0]; w1 = w[1]; w2 = w[2]; w3 = w[3];
    return rowsum16(tadd16(tadd32(c0, c2), tadd32(c1, c3)));
  };
  auto tr_addr = [&](int c, int half) -> u32 {
    int ln = tid & 63; asm volatile("" : "+v"(ln));
    const int row = 8 * (ln >> 4) + 4 * half + ((ln & 15) >> 2), p = ln & 3;
    return lbase + L_WT + row * 1024 + (((8 * wave + 2 * c + (p >> 1)) ^ ((row & 7) << 1)) * 16) + (p & 1) * 8;
  };
  asm volatile("s_waitcnt lgkmcnt(0)" : "+s"(epoch) :: "memory");
  const u32 tag = epoch + 1u;
  const __amdgpu_buffer_rsrc_t rs = __builtin_amdgcn_make_buffer_rsrc(a.ws + X_BASE + (epoch % X_NREG) * X_REGION, 0, X_REGION, 0x00020000);
  if (isA && wave >= 4 && lane == 0) *(volatile LAS u32*)(L + L_FLAG + (wave - 4) * 4) = tag;
  STAMP(1);

  auto store_partial = [&](v4f c0, v4f c1, v4f c2, v4f c3, u32 base, bool act) {
    float v[2];
#pragma unroll
    for (int r = 0; r < 2; ++r) {
      const float e0 = (lane & 4) ? c0[2 + r] : c0[r], e1 = (lane & 4) ? c1[2 + r] : c1[r];
      const float e2 = (lane & 4) ? c2[2 + r] : c2[r], e3 = (lane & 4) ? c3[2 + r] : c3[r];
      const float lo = (lane & 1) ? e1 : e0, hi = (lane & 1) ? e3 : e2;
      v[r] = (lane & 2) ? hi : lo;
    }
    if (act && kr < 8)
      __builtin_amdgcn_raw_buffer_store_b128((v4u){__float_as_uint(v[0]), tag, __float_as_uint(v[1]), tag}, rs,
                                             base + (u32)(wave * 4 + hq) * 128 + gq * 32 + (lane & 4) * 4, 0, 16);
  };

  auto stage_q = [&](u32 xl) {
    const v2u xo = *(const LAS v2u*)(L + L_XS + qkh * 512 + lane * 8), xq = *(const LAS v2u*)(L + L_XS + (qkh ^ 1) * 512 + lane * 8);
    float ac[8];
#pragma unroll
    for (int q = 0; q < 8; ++q) ac[q] = dot2(xo.y, RH[2 + (q >> 2)][q & 3], dot2(xo.x, RH[q >> 2][q & 3], 0.f));
    const float s1 = dot2(xq.y, ONES, dot2(xq.x, ONES, dot2(xo.y, ONES, dot2(xo.x, ONES, 0.f))));
    const float s2 = dot2(xq.y, xq.y, dot2(xq.x, xq.x, dot2(xo.y, xo.y, dot2(xo.x, xo.x, 0.f))));
    const float acc = reduce8(ac, lane);
    float ss = tadd32(s1, s2); ss = tadd16(ss, ss); ss = rowsum16(ss);
    const float mean = rdlane(ss, 0) * (1.f / 512.f);
    const float var = fmaxf(rdlane(ss, 32) * (1.f / 512.f) - mean * mean, 0.f);
    const float y = (acc - mean * r_cs) * __builtin_amdgcn_rsqf(var + 1e-5f);
    if (!isA && (lane & 7) == 0) {
      const v2u d = {__float_as_uint(y), tag};
      __builtin_amdgcn_raw_buffer_store_b64(d, rs, xl + X_E1 + (u32)(g * 16 + jj) * 512 + wave * 64 + lane, 0, 16);
    }
  };

  if (!isA) {
    asm volatile("s_waitcnt vmcnt(0)" ::: "memory");
    lds_barrier();
    v4u r[4];
#pragma unroll
    for (int i = 0; i < 4; ++i) r[i] = *(const LAS v4u*)(L + L_W4 + (i * 64 + lane) * 128 + (wave ^ ((lane >> 1) & 7)) * 16);
#pragma unroll
    for (int p = 0; p < 2; ++p) {
      RH[2 * p] = (v4u){perm_lo(r[2 * p].x, r[2 * p + 1].x), perm_hi(r[2 * p].x, r[2 * p + 1].x), perm_lo(r[2 * p].y, r[2 * p + 1].y), perm_hi(r[2 * p].y, r[2 * p + 1].y)};
      RH[2 * p + 1] = (v4u){perm_lo(r[2 * p].z, r[2 * p + 1].z), perm_hi(r[2 * p].z, r[2 * p + 1].z), perm_lo(r[2 * p].w, r[2 * p + 1].w), perm_hi(r[2 * p].w, r[2 * p + 1].w)};
    }
    float c[8];
#pragma unroll
    for (int q = 0; q < 8; ++q) c[q] = dot2(RH[2 + (q >> 2)][q & 3], ONES, dot2(RH[q >> 2][q & 3], ONES, 0.f));
    r_cs = reduce8(c, lane);
    STAMP(2);
    stage_q(0u);
    STAMP(3);
  }
  {

      const v4u z = {0u, ~tag, 0u, ~tag};
      int t_ = tid; asm volatile("" : "+v"(t_));
      if (isA) {
      } else {
        if ((PREW & 2) && (isC | isH)) __builtin_amdgcn_raw_buffer_store_b128(z, rs, (isC ? X_E3 + (u32)(g * 8 + ci) * 4096 : X_E5 + (u32)hidx * 4096) + (t_ >> 8) * X_LAYER + (t_ & 255) * 16, 0, 16);
        if ((PREW & 2) && t_ < 32) __builtin_amdgcn_raw_buffer_store_b128(z, rs, X_LAYER + X_E1 + (u32)(g * 16 + jj) * 512 + t_ * 16, 0, 16);
        if (isC | isH) {
          lds_barrier();
          int ln = tid & 63; asm volatile("" : "+v"(ln));
          if (isC) {
            __builtin_amdgcn_s_sleep(D_WO);
            const char* p = (const char*)a.wo + (size_t)(g * 512 + ci * 64 + wave) * 1024 + (ln ^ (wave << 1)) * 16;
#pragma unroll
            for (int t = 0; t < 8; ++t) dma16(p + t * 8192, lbase + L_WT + (t * 8 + wave) * 1024);
          } else {
            __builtin_amdgcn_s_sleep(D_W12);
#pragma unroll
            for (int t = 0; t < 4; ++t) {
              const int tt = (t + 2 * (hidx & 1)) & 3, lp = tt * 16 + (ln >> 2);
              dma16((const char*)a.w1 + (size_t)(8 * lp + wave) * 3584 + hidx * 64 + ((ln & 3) ^ ((lp >> 2) & 3)) * 16, lbase + L_W1 + (wave * 4 + tt) * 1024);
            }
            const char* p = (const char*)a.w2 + (size_t)(hidx * 32 + wave) * 1024 + (ln ^ (wave << 1)) * 16;
#pragma unroll
            for (int t = 0; t < 4; ++t) dma16(p + t * 8192, lbase + L_WT + (t * 8 + wave) * 1024);
          }
        }
      }
      }

  for (int layer = 0; layer < 2; ++layer) {
    const u32 xl = layer * X_LAYER;
    if (layer != 0) {
      if (wave == 0) {
        __builtin_amdgcn_s_sleep(D_E6);
        const u32 o0 = X_E6 + (lane >> 1) * 128 + (lane & 1) * 32;
        const u32 off[2] = {o0, o0 + 16};
        v4u v[2];
        const int n = gather<2>(rs, off, tag, v, dead);
        STAMPN(16, n);
        *(LAS v4u*)(L + L_XS + lane * 16) = (v4u){v[0].x, v[0].z, v[1].x, v[1].z};
      }
      lds_barrier();
      stage_q(X_LAYER);
      STAMP(17);
    }

    if (layer != 0 || isA) {
      if (layer != 0) lds_barrier();
      if (layer == 0) {
        if (wave < 4) {
          for (int i = 0; i < 4096; ++i) {
            const v4u f = *(volatile LAS v4u*)(L + L_FLAG);
            if ((f.x == tag) & (f.y == tag) & (f.z == tag) & (f.w == tag)) break;
            __builtin_amdgcn_s_sleep(1);
          }
          if (D_Q1) __builtin_amdgcn_s_sleep(D_Q1);
          const u32 o0 = xl + X_E1 + (u32)(g * 64 + (lane >> 5) * 32 + wave * 8) * 128 + (lane & 31) * 16;
          const u32 off[2] = {o0, o0 + 512};
          v4u v[2];
          const int n = gather<2>(rs, off, tag, v, dead, true);
          STAMPN(4, n);
#pragma unroll
          for (int i = 0; i < 2; ++i) {
            float pA = __uint_as_float(v[i].x), pB = __uint_as_float(v[i].z);
            pA = tadd32(pA, pA); pB = tadd32(pB, pB);
            if (lane < 32) *(LAS u32*)(L + L_QS + (2 * i + (lane >> 4)) * 256 + wave * 64 + (lane & 15) * 4) = pack_bf16(pA, pB);
          }
        } else {
          kv_issue(KV_SPLIT, 32);
          STAMP(4);
        }
      } else {
        __builtin_amdgcn_s_sleep(D_E1);
        const u32 off[1] = {xl + X_E1 + (u32)(g * 64 + (lane >> 5) * 32 + wave * 4) * 128 + (lane & 31) * 16};
        v4u v[1];
        const int n = gather<1>(rs, off, tag, v, dead);
        STAMPN(18, n);
        float pA = __uint_as_float(v[0].x), pB = __uint_as_float(v[0].z);
        pA = tadd32(pA, pA); pB = tadd32(pB, pB);
        if (lane < 32) *(LAS u32*)(L + L_QS + (2 * (wave & 1) + (lane >> 4)) * 256 + (wave >> 1) * 64 + (lane & 15) * 4) = pack_bf16(pA, pB);
      }
      lds_barrier();
      if (layer == 0) {
#pragma unroll
        for (int s = 0; s < 8; ++s) RA[s] = *(const LAS v4u*)(L + L_K + (wave * 32 + (s >> 2) * 16 + kr) * 256 + (((4 * (s & 3) + gq) ^ kr) * 16));
      }
      v4f sa = {0.f, 0.f, 0.f, 0.f}, sb = {0.f, 0.f, 0.f, 0.f};
      {
        v4u qf[4];
#pragma unroll
        for (int s = 0; s < 4; ++s) qf[s] = *(const LAS v4u*)(L + L_QS + s * 256 + hq * 64 + gq * 16);
        __builtin_amdgcn_sched_barrier(0);
#pragma unroll
        for (int s = 0; s < 4; ++s) { sa = MFMA32(RA[s], qf[s], sa); sb = MFMA32(RA[4 + s], qf[s], sb); }
      }
      constexpr float C2 = 0.08838834764831845f * 1.4426950408889634f;
      float ref = 0.f;
      for (int pass = 0;; ++pass) {
        float p[8];
#pragma unroll
        for (int r = 0; r < 4; ++r) { p[r] = __builtin_amdgcn_exp2f(fmaf(sa[r], C2, -ref)); p[4 + r] = __builtin_amdgcn_exp2f(fmaf(sb[r], C2, -ref)); }
        const v4u pf = {pack_bf16(p[0], p[1]), pack_bf16(p[2], p[3]), pack_bf16(p[4], p[5]), pack_bf16(p[6], p[7])};
        v4u pm[4];
#pragma unroll
        for (int c = 0; c < 4; ++c) { const bool on = (kr >> 2) == c; pm[c] = (v4u){on ? pf.x : 0u, on ? pf.y : 0u, on ? pf.z : 0u, on ? pf.w : 0u}; }
        if (layer == 0 && pass == 0) {
          asm volatile("" : "+v"(pm[0]), "+v"(pm[1]), "+v"(pm[2]), "+v"(pm[3]));
        if (wave >= 4) asm volatile("s_waitcnt vmcnt(0)" ::: "memory");
        STAMP(14);
        lds_barrier();
        if (tid == 0) __builtin_amdgcn_raw_buffer_store_b128((v4u){(u32)__builtin_amdgcn_s_memrealtime(), tag, 0u, tag}, rs, X_HINT + (u32)(g * 16 + jj) * 128, 0, 16);
        const int row0 = wave * 32 + 4 * gq + ((lane & 15) >> 2), vp = lane & 3;
        const u32 va = lbase + L_V + row0 * 256 + ((row0 & 7) << 5) + (vp >> 1) * 16 + (vp & 1) * 8;
        v2u t[16];
        asm volatile(
            "ds_read_b64_tr_b16 %0, %16\n\tds_read_b64_tr_b16 %1, %16 offset:4096\n\tds_read_b64_tr_b16 %2, %17\n\tds_read_b64_tr_b16 %3, %17 offset:4096\n\t"
            "ds_read_b64_tr_b16 %4, %18\n\tds_read_b64_tr_b16 %5, %18 offset:4096\n\tds_read_b64_tr_b16 %6, %19\n\tds_read_b64_tr_b16 %7, %19 offset:4096\n\t"
            "ds_read_b64_tr_b16 %8, %20\n\tds_read_b64_tr_b16 %9, %20 offset:4096\n\tds_read_b64_tr_b16 %10, %21\n\tds_read_b64_tr_b16 %11, %21 offset:4096\n\t"
            "ds_read_b64_tr_b16 %12, %22\n\tds_read_b64_tr_b16 %13, %22 offset:4096\n\tds_read_b64_tr_b16 %14, %23\n\tds_read_b64_tr_b16 %15, %23 offset:4096\n\t"
            "s_waitcnt lgkmcnt(0)"
            : "=&v"(t[0]), "=&v"(t[1]), "=&v"(t[2]), "=&v"(t[3]), "=&v"(t[4]), "=&v"(t[5]), "=&v"(t[6]), "=&v"(t[7]), "=&v"(t[8]), "=&v"(t[9]), "=&v"(t[10]),
              "=&v"(t[11]), "=&v"(t[12]), "=&v"(t[13]), "=&v"(t[14]), "=&v"(t[15])
            : "v"(va), "v"(va ^ 32u), "v"(va ^ 64u), "v"(va ^ 96u), "v"(va ^ 128u), "v"(va ^ 160u), "v"(va ^ 192u), "v"(va ^ 224u) : "memory");
#pragma unroll
        for (int u = 0; u < 8; ++u) RB[u] = (v4u){t[2 * u].x, t[2 * u].y, t[2 * u + 1].x, t[2 * u + 1].y};
      }
        v4f oa = {0.f, 0.f, 0.f, 0.f}, ob = {0.f, 0.f, 0.f, 0.f};
#pragma unroll
        for (int c = 0; c < 4; ++c) { oa = MFMA32(pm[c], RB[c], oa); ob = MFMA32(pm[c], RB[4 + c], ob); }
        const v4f lw = MFMA32(pf, ((v4u){ONES, ONES, ONES, ONES}), ((v4f){0.f, 0.f, 0.f, 0.f}));
        const float ml = fmaxf(fmaxf(fmaxf(fmaxf(sa[0], sa[1]), sa[2]), fmaxf(fmaxf(sa[3], sb[0]), sb[1])), fmaxf(sb[2], sb[3]));
        const bool safe = __all(fabsf(ml) <= SAFE_RANGE / C2);
        *(LAS v4f*)(L + L_OW + wave * 2048 + lane * 16) = oa;
        *(LAS v4f*)(L + L_OW + wave * 2048 + 1024 + lane * 16) = ob;
        if (lane == 0) { *(LAS v4f*)(L + L_ML + wave * 16) = lw; *(LAS u32*)(L + L_ML + 128 + wave * 4) = safe ? 0u : 1u; }
        lds_barrier();
        const v4f x0 = *(const LAS v4f*)(L + L_OW + gq * 2048 + wave * 256 + kr * 16), x1 = *(const LAS v4f*)(L + L_OW + 8192 + gq * 2048 + wave * 256 + kr * 16);
        const u32 fl = *(const LAS u32*)(L + L_ML + (lane & 31) * 4 + (lane & 32) * 4);
        if (isA && pass == 0 && __any(lane >= 32 && lane < 40 && fl != 0u)) {
          float m = tmax32(tmax16(ml * C2));
          if (lane < 4) *(LAS float*)(L + L_P + hq * 32 + wave * 4) = m;
          lds_barrier();
          const v4f m0 = *(const LAS v4f*)(L + L_P + hq * 32), m1 = *(const LAS v4f*)(L + L_P + hq * 32 + 16);
          ref = fmaxf(fmaxf(fmaxf(m0[0], m0[1]), fmaxf(m0[2], m0[3])), fmaxf(fmaxf(m1[0], m1[1]), fmaxf(m1[2], m1[3])));
          continue;
        }
        const v4f xs = x0 + x1;
        const float o = tadd16(tadd32(xs[0], xs[2]), tadd32(xs[1], xs[3]));
        const float o1 = dppf<XOR1>(o);
        const u32 pr = pack_bf16(o, o1);
        const u32 pr2 = (u32)__builtin_amdgcn_mov_dpp((int)pr, XOR2, 0xf, 0xf, true);
        const u32 pbase = xl + X_E2 + (u32)((g * 16 + jj) * 17) * 128;
        if (isA && (lane & 3) == 0) {
          const v4u d = {pr, tag, pr2, tag};
          __builtin_amdgcn_raw_buffer_store_b128(d, rs, pbase + (gq * 4 + (wave >> 1)) * 128 + ((wave & 1) * 4 + (kr >> 2)) * 16, 0, 16);
        }
        if (wave == 7) {
          float lt = __uint_as_float(fl);
          lt += dppf<ROR4>(lt); lt += dppf<ROR8>(lt); lt = tadd16(lt, lt);
          if (isA && lane < 4) {
            const v4u d = {__float_as_uint(ref), tag, __float_as_uint(lt), tag};
            __builtin_amdgcn_raw_buffer_store_b128(d, rs, pbase + 16 * 128 + lane * 16, 0, 16);
          }
        }
        break;
      }
      STAMP(5 + layer * 14);
      if (layer == 0 && wave >= 1 && wave < 4) {
        const v4u z = {0u, ~tag, 0u, ~tag};
        int pc = tid - 64; asm volatile("" : "+v"(pc));
        if (pc < 136) __builtin_amdgcn_raw_buffer_store_b128(z, rs, X_LAYER + X_E2 + (u32)((g * 16 + jj) * 17) * 128 + pc * 16, 0, 16);
        else if (isR && pc < 160) { const int line = (pc - 136) >> 3;
          __builtin_amdgcn_raw_buffer_store_b128(z, rs, (line < 2 ? line * X_LAYER + X_E4 : X_E6) + (u32)ri * 128 + (pc & 7) * 16, 0, 16); }
      }
    }

    if (layer == 0 && (isC | isH)) {
      int ln = tid & 63; asm volatile("" : "+v"(ln));
          asm volatile("s_waitcnt vmcnt(0)" ::: "memory");
          lds_barrier();
          {
            v2u t[16];
            const u32 a0 = tr_addr(0, 0), a1 = tr_addr(0, 1), a2 = tr_addr(1, 0), a3 = tr_addr(1, 1), a4 = tr_addr(2, 0), a5 = tr_addr(2, 1), a6 = tr_addr(3, 0), a7 = tr_addr(3, 1);
            asm volatile(
                "ds_read_b64_tr_b16 %0, %16\n\tds_read_b64_tr_b16 %1, %17\n\tds_read_b64_tr_b16 %2, %16 offset:32768\n\tds_read_b64_tr_b16 %3, %17 offset:32768\n\t"
                "ds_read_b64_tr_b16 %4, %18\n\tds_read_b64_tr_b16 %5, %19\n\tds_read_b64_tr_b16 %6, %18 offset:32768\n\tds_read_b64_tr_b16 %7, %19 offset:32768\n\t"
                "ds_read_b64_tr_b16 %8, %20\n\tds_read_b64_tr_b16 %9, %21\n\tds_read_b64_tr_b16 %10, %20 offset:32768\n\tds_read_b64_tr_b16 %11, %21 offset:32768\n\t"
                "ds_read_b64_tr_b16 %12, %22\n\tds_read_b64_tr_b16 %13, %23\n\tds_read_b64_tr_b16 %14, %22 offset:32768\n\tds_read_b64_tr_b16 %15, %23 offset:32768\n\t"
                "s_waitcnt lgkmcnt(0)"
                : "=&v"(t[0]), "=&v"(t[1]), "=&v"(t[2]), "=&v"(t[3]), "=&v"(t[4]), "=&v"(t[5]), "=&v"(t[6]), "=&v"(t[7]), "=&v"(t[8]), "=&v"(t[9]), "=&v"(t[10]),
                  "=&v"(t[11]), "=&v"(t[12]), "=&v"(t[13]), "=&v"(t[14]), "=&v"(t[15])
                : "v"(a0), "v"(a1), "v"(a2), "v"(a3), "v"(a4), "v"(a5), "v"(a6), "v"(a7) : "memory");
#pragma unroll
            for (int c = 0; c < 4; ++c) {
              RB[2 * c] = (v4u){t[4 * c].x, t[4 * c].y, t[4 * c + 1].x, t[4 * c + 1].y};
              RB[2 * c + 1] = (v4u){t[4 * c + 2].x, t[4 * c + 2].y, t[4 * c + 3].x, t[4 * c + 3].y};
            }
          }
          if (isH) {
            h_cs = prep4(L_W1, RA[0], RA[1], RA[2], RA[3]);
            h_bias = bf_lo(*(const LAS u16*)(L + L_B + (wave * 4 + (ln >> 4)) * 2));
          }
          STAMP(28);
    }
    const bool late = (layer == 0) & isH;
    if (!late) {
      if (wave == 0) {
        if (layer == 0) {
          if (!isA && !isC && !isH) __builtin_amdgcn_s_sleep(D_G0);
          const u32 ho[2] = {X_HINT + (u32)lane * 128, X_HINT + (u32)lane * 128 + 8192};
          v4u hv[2];
          const int nh = gather<2>(rs, ho, tag, hv, dead, true);
          STAMPN(15, nh);
          if (D_H) __builtin_amdgcn_s_sleep(D_H);
        } else {
          __builtin_amdgcn_s_sleep(D_E2);
        }
        u32 off[8];
#pragma unroll
        for (int i = 0; i < 4; ++i) {
          const u32 pb = xl + X_E2 + (u32)((g * 16 + 4 * i + gq) * 17) * 128;
          off[i] = pb + ci * 256 + kr * 16;
          off[4 + i] = pb + 16 * 128 + (ci >> 1) * 16;
        }
        v4u v[8];
        const int n = gather<8>(rs, off, tag, v, dead, layer == 0);
        STAMPN(6 + layer * 14, n);
        float M = vmax(vmax3(__uint_as_float(v[4].x), __uint_as_float(v[5].x), __uint_as_float(v[6].x)), __uint_as_float(v[7].x));
        M = tmax16(M); M = tmax32(M);
        float a0 = 0.f, a1 = 0.f, a2 = 0.f, a3 = 0.f, Ls = 0.f;
#pragma unroll
        for (int i = 0; i < 4; ++i) {
          const float e = __builtin_amdgcn_exp2f(__uint_as_float(v[4 + i].x) - M);
          Ls = fmaf(e, __uint_as_float(v[4 + i].z), Ls);
          a0 = fmaf(e, bf_lo(v[i].x), a0); a1 = fmaf(e, bf_hi(v[i].x), a1); a2 = fmaf(e, bf_lo(v[i].z), a2); a3 = fmaf(e, bf_hi(v[i].z), a3);
        }
        float b0 = tadd32(a0, a2), b1 = tadd32(a1, a3); Ls = tadd32(Ls, Ls);
        b0 = tadd16(b0, b0); b1 = tadd16(b1, b1); Ls = tadd16(Ls, Ls);
        const float inv = __builtin_amdgcn_rcpf(Ls);
        if (!(lane & 16)) *(LAS u32*)(L + L_OS + kr * 8 + (lane >> 5) * 4) = pack_bf16(b0 * inv, b1 * inv);
      }
      lds_barrier();
      const v4u of0 = *(const LAS v4u*)(L + L_OS + gq * 16), of1 = *(const LAS v4u*)(L + L_OS + 64 + gq * 16);
      v4f acc[4];
#pragma unroll
      for (int c = 0; c < 4; ++c) acc[c] = MFMA32(RB[2 * c + 1], of1, MFMA32(RB[2 * c], of0, ((v4f){0.f, 0.f, 0.f, 0.f})));
      store_partial(acc[0], acc[1], acc[2], acc[3], xl + X_E3 + (u32)(g * 8 + ci) * 4096, isC);
      STAMP(7 + layer * 14);
    }

#pragma unroll
    for (int ph = 0; ph < 2; ++ph) {
      lds_barrier();
      if (wave == 0 && !(late && ph == 0)) {
        constexpr int NP = 8;
        if (ph == 0) __builtin_amdgcn_s_sleep(D_E3); else __builtin_amdgcn_s_sleep(D_E5);
        u32 off[NP];
#pragma unroll
        for (int i = 0; i < NP; ++i) off[i] = xl + (ph == 0 ? X_E3 : X_E5) + (u32)(((ph == 1 && i == 7 ? 6 : i) * 8 + (lane >> 3)) * 32 + ri) * 128 + (lane & 7) * 16;
        const u32 xin = ph == 0 ? *(const LAS u32*)(L + L_XS + ri * 32 + (lane & 7) * 4) : xkeep;
        const u32 bb = *(const LAS u32*)(L + L_B + 64 + (lane & 7) * 4);
        float s0, s1;
        int n = 0;
        for (;; ++n) {
          asm volatile("" ::: "memory");
          v4u v[NP];
#pragma unroll
          for (int i = 0; i < (ph == 0 ? 8 : 7); ++i) v[i] = __builtin_amdgcn_raw_buffer_load_b128(rs, off[i], 0, 16);
          u32 bad = 0u; s0 = 0.f; s1 = 0.f;
#pragma unroll
          for (int i = 0; i < (ph == 0 ? 8 : 7); ++i) {
            bad |= (v[i].y ^ tag); bad |= (v[i].w ^ tag); s0 += __uint_as_float(v[i].x); s1 += __uint_as_float(v[i].z);
            asm volatile("" : "+v"(bad), "+v"(s0), "+v"(s1));
          }
          if (__all(bad == 0u)) break;
          if (dead || n > SPIN_CAP) { dead = true; break; }
        }
        STAMPN(8 + ph * 4 + layer * 14, n);
        float t = tadd32(s0, s1); t = tadd16(t, t); t += dppf<ROR8>(t);
        float r = rbf(t);
        if (ph == 1) r = rbf(r + ((lane & 32) ? bf_hi(bb) : bf_lo(bb)));
        r = r + ((lane & 32) ? bf_hi(xin) : bf_lo(xin));
        auto sw = __builtin_amdgcn_permlane32_swap(__float_as_uint(r), __float_as_uint(r), false, false);
        const u32 pr = pack_bf16(__uint_as_float(sw[0]), __uint_as_float(sw[1]));
        xkeep = pr;
        if (isR && lane < 8) {
          if (ph == 0 || layer == 0) {
            const v2u d = {pr, tag};
            __builtin_amdgcn_raw_buffer_store_b64(d, rs, xl + (ph == 0 ? X_E4 : X_E6) + (u32)ri * 128 + lane * 8, 0, 16);
          } else {
            *(u32*)(a.out + ri * 16 + lane * 2) = pr;
          }
        }
        STAMP(9 + ph * 4 + layer * 14);
      }
      if (ph == 0) {
        if (wave == 0) {
          if (late) {
            const u32 ho[2] = {X_HINT + (u32)lane * 128, X_HINT + (u32)lane * 128 + 8192};
            v4u hv[2];
            gather<2>(rs, ho, tag, hv, dead, true);
            const u32 now = (u32)__builtin_amdgcn_s_memrealtime();
            float age = fminf((float)(int)(now - hv[0].x), (float)(int)(now - hv[1].x));
            age = fminf(age, dppf<ROR8>(age)); age = fminf(age, dppf<ROR4>(age)); age = fminf(age, dppf<ROR2>(age)); age = fminf(age, dppf<ROR1>(age));
            age = -tmax32(tmax16(-age));
            const int wait = T_H4 - (int)rdlane(age, 0);
            for (int i = 0; i < 4096 && (int)((u32)__builtin_amdgcn_s_memrealtime() - now) < wait; ++i) {}
          } else {
            __builtin_amdgcn_s_sleep(D_E4);
          }
          const u32 o0 = xl + X_E4 + (lane >> 1) * 128 + (lane & 1) * 32;
          const u32 off[2] = {o0, o0 + 16};
          v4u v[2];
          const int n = gather<2>(rs, off, tag, v, dead);
          STAMPN(10 + layer * 14, n);
          *(LAS v4u*)(L + L_XP + lane * 16) = (v4u){v[0].x, v[0].z, v[1].x, v[1].z};
        }
        lds_barrier();
        const v4u xv = *(const LAS v4u*)(L + L_XP + lane * 16);
        const v4u w1r[4] = {RA[0], RA[1], RA[2], RA[3]};
        const float z = rbf(rbf(ln_gemv4(xv, w1r, h_cs)) + h_bias);
        const float u2 = z * (1.5957691216057308f + 0.07135481627260025f * z * z);
        const float y = z * __builtin_amdgcn_rcpf(1.f + __builtin_amdgcn_exp2f(-1.4426950408889634f * u2));
        if (kr == 0) *(LAS u16*)(L + L_HS + (wave * 4 + gq) * 2) = (u16)pack_bf16(y, y);
        lds_barrier();
        const v4u hf = *(const LAS v4u*)(L + L_HS + gq * 16);
        v4f acc[4];
#pragma unroll
        for (int c = 0; c < 4; ++c) acc[c] = MFMA32(RB[2 * c], hf, ((v4f){0.f, 0.f, 0.f, 0.f}));
        store_partial(acc[0], acc[1], acc[2], acc[3], xl + X_E5 + (u32)hidx * 4096, isH);
        STAMP(11 + layer * 14);
      }
    }
  }
  if (b == 0 && tid == 0) __hip_atomic_store((u32*)a.ws, tag, __ATOMIC_RELAXED, __HIP_MEMORY_SCOPE_AGENT);
}

}

extern "C" void kernel_launch(void* const* d_in, const int* in_sizes, int n_in, void* d_out, int out_size, void* d_ws,
                              size_t ws_size, hipStream_t stream) {
  Args a;
  a.x = (const u16*)d_in[0]; a.kc = (const u16*)d_in[1]; a.vc = (const u16*)d_in[2]; a.wq = (const u16*)d_in[3];
  a.wo = (const u16*)d_in[4]; a.w1 = (const u16*)d_in[6]; a.b1 = (const u16*)d_in[7]; a.w2 = (const u16*)d_in[8];
  a.b2 = (const u16*)d_in[9]; a.out = (u16*)d_out; a.ws = (unsigned char*)d_ws;
  fused_decode<<<NWG, NTHR, 0, stream>>>(a);
}
```

```cpp
#include <hip/hip_runtime.h>
#include <stdint.h>

typedef unsigned u32;
typedef unsigned short u16;
typedef unsigned long long u64;
typedef u32 v4u __attribute__((ext_vector_type(4)));
typedef u32 v2u __attribute__((ext_vector_type(2)));
typedef float v4f __attribute__((ext_vector_type(4)));
typedef __bf16 v8bf __attribute__((ext_vector_type(8)));
typedef __bf16 v2bf __attribute__((ext_vector_type(2)));
#define LAS __attribute__((address_space(3)))

namespace {

constexpr int D_E1 = 6, D_E2 = 6, D_E3 = 12, D_E4 = 11, D_E5 = 11, D_E6 = 11;
constexpr int D_KV0 = 8;
constexpr int KV_WIN = 14, KV_SPLIT = 24;
constexpr int D_Q1 = 10;
constexpr int D_WO = 12, D_W12 = 70;
constexpr int D_G0 = 64, D_BACK = 2;
constexpr int T_H4 = 305;
constexpr float SAFE_RANGE = 60.f;
static_assert(KV_WIN <= 16, "K must have landed when the last V load is issued");

constexpr int SK = 65536, WIN = 4096;
constexpr int NWG = 256, NTHR = 512;

constexpr u32 L_K = 0, L_V = 65536, L_W4 = 0, L_WT = 32768, L_W1 = 65536;
constexpr u32 L_QS = 131072, L_XS = 132096, L_XP = 133120, L_P = 134144, L_ML = 136192, L_OS = 136448, L_HS = 136704,
              L_B = 136960, L_FLAG = 137984, L_SCR = 138240, L_OW = 141312, L_END = 141312 + 16384;

constexpr u32 X_E1 = 0;
constexpr u32 X_E2 = X_E1 + 512 * 128;
constexpr u32 X_E3 = X_E2 + 2176 * 128;
constexpr u32 X_E4 = X_E3 + 2048 * 128;
constexpr u32 X_E5 = X_E4 + 32 * 128;
constexpr u32 X_E6 = X_E5 + 1792 * 128;
constexpr u32 X_LAYER = X_E6 + 32 * 128;
constexpr u32 X_HINT = 2 * X_LAYER;
constexpr u32 X_REGION = 2u << 20;
constexpr u32 X_NREG = 64;
constexpr u32 X_BASE = 2u << 20;
static_assert(2 * X_LAYER + 128 * 128 <= X_REGION, "region");

constexpr u32 CODE_BYTES = 15488;
constexpr int SPIN_CAP = 1 << 18;

__device__ __forceinline__ void dma16(const void* gsrc, u32 lds_dst) {
  u32 keep;
  asm volatile("s_mov_b32 %0, m0\n\ts_mov_b32 m0, %2\n\ts_nop 0\n\tglobal_load_lds_dwordx4 %1, off\n\ts_mov_b32 m0, %0"
               : "=&s"(keep) : "v"(gsrc), "s"(__builtin_amdgcn_readfirstlane(lds_dst)) : "memory");
}
__device__ __forceinline__ void lds_barrier() {
  asm volatile("s_waitcnt lgkmcnt(0)\n\ts_barrier" ::: "memory");
}
__device__ __forceinline__ float dot2(u32 a, u32 b, float c) {
  return __builtin_amdgcn_fdot2_f32_bf16(__builtin_bit_cast(v2bf, a), __builtin_bit_cast(v2bf, b), c, false);
}
__device__ __forceinline__ u32 pack_bf16(float lo, float hi) {
  typedef float v2f __attribute__((ext_vector_type(2)));
  return __builtin_bit_cast(u32, __builtin_convertvector((v2f){lo, hi}, v2bf));
}
__device__ __forceinline__ float bf_lo(u32 p) { return __uint_as_float(p << 16); }
__device__ __forceinline__ float bf_hi(u32 p) { return __uint_as_float(p & 0xffff0000u); }
__device__ __forceinline__ float rbf(float x) { return bf_lo(pack_bf16(x, x)); }
__device__ __forceinline__ u32 perm_lo(u32 a, u32 b) { return __builtin_amdgcn_perm(b, a, 0x05040100u); }
__device__ __forceinline__ u32 perm_hi(u32 a, u32 b) { return __builtin_amdgcn_perm(b, a, 0x07060302u); }
__device__ __forceinline__ float rdlane(float x, int l) { return __builtin_bit_cast(float, __builtin_amdgcn_readlane(__builtin_bit_cast(int, x), l)); }

template <int CTRL> __device__ __forceinline__ float dppf(float x) {
  return __builtin_bit_cast(float, __builtin_amdgcn_mov_dpp(__builtin_bit_cast(int, x), CTRL, 0xf, 0xf, true));
}
constexpr int XOR1 = 0xB1, XOR2 = 0x4E, XOR7 = 0x141, ROR8 = 0x128, ROR4 = 0x124, ROR2 = 0x122, ROR1 = 0x121;
constexpr u32 ONES = 0x3f803f80u;

__device__ __forceinline__ float tadd32(float a, float b) {
  auto s = __builtin_amdgcn_permlane32_swap(__float_as_uint(a), __float_as_uint(b), false, false);
  return __uint_as_float(s[0]) + __uint_as_float(s[1]);
}
__device__ __forceinline__ float tadd16(float a, float b) {
  auto s = __builtin_amdgcn_permlane16_swap(__float_as_uint(a), __float_as_uint(b), false, false);
  return __uint_as_float(s[0]) + __uint_as_float(s[1]);
}
__device__ __forceinline__ float vmax(float a, float b) { return __builtin_amdgcn_fmed3f(a, b, __builtin_inff()); }
__device__ __forceinline__ float vmax3(float a, float b, float c) { return vmax(vmax(a, b), c); }
__device__ __forceinline__ float tmax32(float a) {
  auto s = __builtin_amdgcn_permlane32_swap(__float_as_uint(a), __float_as_uint(a), false, false);
  return vmax(__uint_as_float(s[0]), __uint_as_float(s[1]));
}
__device__ __forceinline__ float tmax16(float a) {
  auto s = __builtin_amdgcn_permlane16_swap(__float_as_uint(a), __float_as_uint(a), false, false);
  return vmax(__uint_as_float(s[0]), __uint_as_float(s[1]));
}
__device__ __forceinline__ float rowsum16(float x) {
  x += dppf<ROR8>(x); x += dppf<ROR4>(x); x += dppf<ROR2>(x); x += dppf<ROR1>(x);
  return x;
}
__device__ __forceinline__ float reduce8(const float (&a)[8], int lane) {
  float t4[4], t2[2];
#pragma unroll
  for (int c = 0; c < 4; ++c) t4[c] = tadd32(a[c], a[c + 4]);
#pragma unroll
  for (int c = 0; c < 2; ++c) t2[c] = tadd16(t4[c], t4[c + 2]);
  const float lo = t2[0] + dppf<ROR8>(t2[0]), hi = t2[1] + dppf<ROR8>(t2[1]);
  float r = (lane & 8) ? hi : lo;
  r += dppf<XOR7>(r); r += dppf<XOR2>(r); r += dppf<XOR1>(r);
  return r;
}

template <int N>
__device__ __forceinline__ int gather(__amdgpu_buffer_rsrc_t rs, const u32 (&off)[N], u32 tag, v4u (&v)[N], bool& dead, bool slow = false) {
  int spin = 0;
  for (;; ++spin) {
    asm volatile("" ::: "memory");
#pragma unroll
    for (int i = 0; i < N; ++i) v[i] = __builtin_amdgcn_raw_buffer_load_b128(rs, off[i], 0, 16);
    u32 bad = 0u;
#pragma unroll
    for (int i = 0; i < N; ++i) { bad |= (v[i].y ^ tag); bad |= (v[i].w ^ tag); asm volatile("" : "+v"(bad)); }
    if (__all(bad == 0u)) break;
    if (dead || spin > SPIN_CAP) { dead = true; break; }
    if (slow) __builtin_amdgcn_s_sleep(D_BACK);
  }
  return spin;
}

struct Args {
  const u16* x; const u16* kc; const u16* vc; const u16* wq; const u16* wo;
  const u16* w1; const u16* b1; const u16* w2; const u16* b2;
  u16* out; unsigned char* ws;
};

#define MFMA32(A, B, C) __builtin_amdgcn_mfma_f32_16x16x32_bf16(__builtin_bit_cast(v8bf, A), __builtin_bit_cast(v8bf, B), C, 0, 0, 0)

__device__ __forceinline__ float ln_gemv4(const v4u xv, const v4u (&w)[4], float cs) {
  float a0 = 0.f, a1 = 0.f, a2 = 0.f, a3 = 0.f, s1 = 0.f, s2 = 0.f;
#pragma unroll
  for (int p = 0; p < 4; ++p) {
    a0 = dot2(xv[p], w[p].x, a0); a1 = dot2(xv[p], w[p].y, a1); a2 = dot2(xv[p], w[p].z, a2); a3 = dot2(xv[p], w[p].w, a3);
    s1 = dot2(xv[p], ONES, s1); s2 = dot2(xv[p], xv[p], s2);
  }
  const float acc = rowsum16(tadd16(tadd32(a0, a2), tadd32(a1, a3)));
  float ss = tadd32(s1, s2); ss = tadd16(ss, ss); ss = rowsum16(ss);
  const float mean = rdlane(ss, 0) * (1.f / 512.f);
  const float var = fmaxf(rdlane(ss, 32) * (1.f / 512.f) - mean * mean, 0.f);
  const float rstd = __builtin_amdgcn_rsqf(var + 1e-5f);
  return (acc - mean * cs) * rstd;
}

__global__ void __launch_bounds__(NTHR, 2) fused_decode(Args a) {
  __shared__ __attribute__((aligned(256))) unsigned char smem[L_END];
  LAS unsigned char* L = (LAS unsigned char*)smem;
  const u32 lbase = (u32)(size_t)L;
  const int tid = threadIdx.x, lane = tid & 63, wave = __builtin_amdgcn_readfirstlane(tid >> 6);
  const int b = blockIdx.x, g = b & 7, j = b >> 3;

  const bool isA = j < 16, isR = j < 4, isC = (j >= 16) & (j < 24), isH = (j >= 24) & (j < 31);
  const int jj = j & 15;
  const int qkh = (j >> 3) & 1, qcg = j & 7;
  const int ci = j & 7;
  const int hidx = g * 7 + (isH ? j - 24 : 0);
  const int ri = 4 * g + (j & 3);

  {
    u64 pc; asm volatile("s_getpc_b64 %0" : "=s"(pc));
    if (wave >= 1 && wave <= 3) {
      const u32 o = (u32)(wave - 1) * 8192u + (u32)lane * 128u;
      dma16((const char*)((pc & ~127ull) + (o < CODE_BYTES ? o : 0u)), lbase + L_SCR + (wave - 1) * 1024);
    }
  }
  u32 epoch;
  asm volatile("s_load_dword %0, %1, 0x0" : "=s"(epoch) : "s"(a.ws) : "memory");
  auto kv_issue = [&](int t0, int t1) {
    int ln = tid & 63; asm volatile("" : "+v"(ln));
    const int w4 = wave - 4;
    const size_t kvoff = ((size_t)g * SK + (SK - WIN) + jj * 256) * 256;
    const int r0 = w4 * 4 + (ln >> 4);
    const char* kp = (const char*)a.kc + kvoff + r0 * 256 + ((ln & 15) ^ r0) * 16;
    const char* vp = (const char*)a.vc + kvoff + r0 * 256 + ((ln & 15) ^ ((r0 & 7) << 1)) * 16 - 65536;
#pragma unroll 1
    for (int t = t0; t < t1; ++t) {
      asm volatile("s_waitcnt vmcnt(%0)" :: "n"(KV_WIN - 1) : "memory");
      dma16((t < 16 ? kp : vp) + t * 4096, lbase + (t * 4 + w4) * 1024);
    }
  };
  if (isA) {
    if (wave >= 4) { __builtin_amdgcn_s_sleep(D_KV0); kv_issue(0, KV_SPLIT); }
  } else {
#pragma unroll
    for (int t = 0; t < 4; ++t) {
      const int lp = wave * 8 + (lane >> 3);
      dma16((const char*)a.wq + (size_t)(256 * qkh + 4 * lp + t) * 8192 + g * 1024 + qcg * 128 + ((lane & 7) ^ ((lp >> 1) & 7)) * 16, lbase + L_W4 + (t * 8 + wave) * 1024);
    }
  }
  if (wave == 0) dma16((const char*)a.x + lane * 16, lbase + L_XS);
  if (wave == 1) {
    const char* src = (lane & 7) < 4 ? (const char*)a.b1 + hidx * 64 + (lane & 3) * 16 : (const char*)a.b2 + ri * 32 + (lane & 1) * 16;
    dma16(src, lbase + L_B);
  }

  bool dead = false;
  const int gq = lane >> 4, hq = lane & 3, kr = lane & 15;
  v4u RA[8], RB[8], RH[4];
  float r_cs = 0.f, h_cs = 0.f, h_bias = 0.f;
  u32 xkeep = 0u;
#pragma unroll
  for (int i = 0; i < 8; ++i) { RA[i] = (v4u){0u, 0u, 0u, 0u}; RB[i] = (v4u){0u, 0u, 0u, 0u}; }
#pragma unroll
  for (int i = 0; i < 4; ++i) RH[i] = (v4u){0u, 0u, 0u, 0u};

  auto prep4 = [&](u32 base, v4u& w0, v4u& w1, v4u& w2, v4u& w3) -> float {
    int ln = tid & 63; asm volatile("" : "+v"(ln));
    v2u r[8];
#pragma unroll
    for (int i = 0; i < 8; ++i) r[i] = *(const LAS v2u*)(L + base + (i * 64 + ln) * 64 + ((wave >> 1) ^ ((ln >> 2) & 3)) * 16 + (wave & 1) * 8);
    v4u w[4];
#pragma unroll
    for (int p = 0; p < 4; ++p)
      w[p] = (v4u){perm_lo(r[2 * p].x, r[2 * p + 1].x), perm_hi(r[2 * p].x, r[2 * p + 1].x), perm_lo(r[2 * p].y, r[2 * p + 1].y), perm_hi(r[2 * p].y, r[2 * p + 1].y)};
    float c0 = 0.f, c1 = 0.f, c2 = 0.f, c3 = 0.f;
#pragma unroll
    for (int p = 0; p < 4; ++p) { c0 = dot2(w[p].x, ONES, c0); c1 = dot2(w[p].y, ONES, c1); c2 = dot2(w[p].z, ONES, c2); c3 = dot2(w[p].w, ONES, c3); }
    w0 = w[0]; w1 = w[1]; w2 = w[2]; w3 = w[3];
    return rowsum16(tadd16(tadd32(c0, c2), tadd32(c1, c3)));
  };
  auto tr_addr = [&](int c, int half) -> u32 {
    int ln = tid & 63; asm volatile("" : "+v"(ln));
    const int row = 8 * (ln >> 4) + 4 * half + ((ln & 15) >> 2), p = ln & 3;
    return lbase + L_WT + row * 1024 + (((8 * wave + 2 * c + (p >> 1)) ^ ((row & 7) << 1)) * 16) + (p & 1) * 8;
  };
  asm volatile("s_waitcnt lgkmcnt(0)" : "+s"(epoch) :: "memory");
  const u32 tag = epoch + 1u;
  const __amdgpu_buffer_rsrc_t rs = __builtin_amdgcn_make_buffer_rsrc(a.ws + X_BASE + (epoch % X_NREG) * X_REGION, 0, X_REGION, 0x00020000);
  if (isA && wave >= 4 && lane == 0) *(volatile LAS u32*)(L + L_FLAG + (wave - 4) * 4) = tag;

  auto store_partial = [&](float c0, float c1, float c2, float c3, u32 base, bool act) {
    const float lo = (lane & 16) ? c1 : c0, hi = (lane & 16) ? c3 : c2;
    float v = (lane & 32) ? hi : lo;
    float v1 = dppf<XOR1>(v);
    asm volatile("" : "+v"(v), "+v"(v1));
    if (act && !(lane & 1))
      __builtin_amdgcn_raw_buffer_store_b128((v4u){__float_as_uint(v), tag, __float_as_uint(v1), tag}, rs, base + (u32)wave * 512 + lane * 8, 0, 16);
  };

  auto stage_q = [&](u32 xl) {
    const v2u xo = *(const LAS v2u*)(L + L_XS + qkh * 512 + lane * 8), xq = *(const LAS v2u*)(L + L_XS + (qkh ^ 1) * 512 + lane * 8);
    float ac[8];
#pragma unroll
    for (int q = 0; q < 8; ++q) ac[q] = dot2(xo.y, RH[2 + (q >> 2)][q & 3], dot2(xo.x, RH[q >> 2][q & 3], 0.f));
    const float s1 = dot2(xq.y, ONES, dot2(xq.x, ONES, dot2(xo.y, ONES, dot2(xo.x, ONES, 0.f))));
    const float s2 = dot2(xq.y, xq.y, dot2(xq.x, xq.x, dot2(xo.y, xo.y, dot2(xo.x, xo.x, 0.f))));
    const float acc = reduce8(ac, lane);
    float ss = tadd32(s1, s2); ss = tadd16(ss, ss); ss = rowsum16(ss);
    const float mean = rdlane(ss, 0) * (1.f / 512.f);
    const float var = fmaxf(rdlane(ss, 32) * (1.f / 512.f) - mean * mean, 0.f);
    const float y = (acc - mean * r_cs) * __builtin_amdgcn_rsqf(var + 1e-5f);
    if (!isA && (lane & 7) == 0) {
      const v2u d = {__float_as_uint(y), tag};
      __builtin_amdgcn_raw_buffer_store_b64(d, rs, xl + X_E1 + (u32)(g * 16 + jj) * 512 + wave * 64 + lane, 0, 16);
    }
  };

  if (!isA) {
    asm volatile("s_waitcnt vmcnt(0)" ::: "memory");
    lds_barrier();
    v4u r[4];
#pragma unroll
    for (int i = 0; i < 4; ++i) r[i] = *(const LAS v4u*)(L + L_W4 + (i * 64 + lane) * 128 + (wave ^ ((lane >> 1) & 7)) * 16);
#pragma unroll
    for (int p = 0; p < 2; ++p) {
      RH[2 * p] = (v4u){perm_lo(r[2 * p].x, r[2 * p + 1].x), perm_hi(r[2 * p].x, r[2 * p + 1].x), perm_lo(r[2 * p].y, r[2 * p + 1].y), perm_hi(r[2 * p].y, r[2 * p + 1].y)};
      RH[2 * p + 1] = (v4u){perm_lo(r[2 * p].z, r[2 * p + 1].z), perm_hi(r[2 * p].z, r[2 * p + 1].z), perm_lo(r[2 * p].w, r[2 * p + 1].w), perm_hi(r[2 * p].w, r[2 * p + 1].w)};
    }
    float c[8];
#pragma unroll
    for (int q = 0; q < 8; ++q) c[q] = dot2(RH[2 + (q >> 2)][q & 3], ONES, dot2(RH[q >> 2][q & 3], ONES, 0.f));
    r_cs = reduce8(c, lane);
    stage_q(0u);
  }
  {

      const v4u z = {0u, ~tag, 0u, ~tag};
      int t_ = tid; asm volatile("" : "+v"(t_));
      if (!isA) {
        if (isC | isH) __builtin_amdgcn_raw_buffer_store_b128(z, rs, (isC ? X_E3 + (u32)(g * 8 + ci) * 4096 : X_E5 + (u32)hidx * 4096) + (t_ >> 8) * X_LAYER + (t_ & 255) * 16, 0, 16);
        if (t_ < 32) __builtin_amdgcn_raw_buffer_store_b128(z, rs, X_LAYER + X_E1 + (u32)(g * 16 + jj) * 512 + t_ * 16, 0, 16);
        if (isC | isH) {
          lds_barrier();
          int ln = tid & 63; asm volatile("" : "+v"(ln));
          if (isC) {
            __builtin_amdgcn_s_sleep(D_WO);
            const char* p = (const char*)a.wo + (size_t)(g * 512 + ci * 64 + wave) * 1024 + (ln ^ (wave << 1)) * 16;
#pragma unroll
            for (int t = 0; t < 8; ++t) dma16(p + t * 8192, lbase + L_WT + (t * 8 + wave) * 1024);
          } else {
            __builtin_amdgcn_s_sleep(D_W12);
#pragma unroll
            for (int t = 0; t < 4; ++t) {
              const int tt = (t + 2 * (hidx & 1)) & 3, lp = tt * 16 + (ln >> 2);
              dma16((const char*)a.w1 + (size_t)(8 * lp + wave) * 3584 + hidx * 64 + ((ln & 3) ^ ((lp >> 2) & 3)) * 16, lbase + L_W1 + (wave * 4 + tt) * 1024);
            }
            const char* p = (const char*)a.w2 + (size_t)(hidx * 32 + wave) * 1024 + (ln ^ (wave << 1)) * 16;
#pragma unroll
            for (int t = 0; t < 4; ++t) dma16(p + t * 8192, lbase + L_WT + (t * 8 + wave) * 1024);
          }
        }
      }
      }

  for (int layer = 0; layer < 2; ++layer) {
    const u32 xl = layer * X_LAYER;
    if (layer != 0) {
      if (wave == 0) {
        __builtin_amdgcn_s_sleep(D_E6);
        const u32 o0 = X_E6 + (lane >> 1) * 128 + (lane & 1) * 32;
        const u32 off[2] = {o0, o0 + 16};
        v4u v[2];
        gather<2>(rs, off, tag, v, dead);
        *(LAS v4u*)(L + L_XS + lane * 16) = (v4u){v[0].x, v[0].z, v[1].x, v[1].z};
      }
      lds_barrier();
      stage_q(X_LAYER);
    }

    if (layer != 0 || isA) {
      if (layer != 0) lds_barrier();
      if (layer == 0) {
        if (wave < 4) {
          for (int i = 0; i < 4096; ++i) {
            const v4u f = *(volatile LAS v4u*)(L + L_FLAG);
            if ((f.x == tag) & (f.y == tag) & (f.z == tag) & (f.w == tag)) break;
            __builtin_amdgcn_s_sleep(1);
          }
          __builtin_amdgcn_s_sleep(D_Q1);
          const u32 o0 = xl + X_E1 + (u32)(g * 64 + (lane >> 5) * 32 + wave * 8) * 128 + (lane & 31) * 16;
          const u32 off[2] = {o0, o0 + 512};
          v4u v[2];
          gather<2>(rs, off, tag, v, dead, true);
#pragma unroll
          for (int i = 0; i < 2; ++i) {
            float pA = __uint_as_float(v[i].x), pB = __uint_as_float(v[i].z);
            pA = tadd32(pA, pA); pB = tadd32(pB, pB);
            if (lane < 32) *(LAS u32*)(L + L_QS + (2 * i + (lane >> 4)) * 256 + wave * 64 + (lane & 15) * 4) = pack_bf16(pA, pB);
          }
        } else {
          kv_issue(KV_SPLIT, 32);
        }
      } else {
        __builtin_amdgcn_s_sleep(D_E1);
        const u32 off[1] = {xl + X_E1 + (u32)(g * 64 + (lane >> 5) * 32 + wave * 4) * 128 + (lane & 31) * 16};
        v4u v[1];
        gather<1>(rs, off, tag, v, dead);
        float pA = __uint_as_float(v[0].x), pB = __uint_as_float(v[0].z);
        pA = tadd32(pA, pA); pB = tadd32(pB, pB);
        if (lane < 32) *(LAS u32*)(L + L_QS + (2 * (wave & 1) + (lane >> 4)) * 256 + (wave >> 1) * 64 + (lane & 15) * 4) = pack_bf16(pA, pB);
      }
      lds_barrier();
      if (layer == 0) {
#pragma unroll
        for (int s = 0; s < 8; ++s) RA[s] = *(const LAS v4u*)(L + L_K + (wave * 32 + (s >> 2) * 16 + kr) * 256 + (((4 * (s & 3) + gq) ^ kr) * 16));
      }
      v4f sa = {0.f, 0.f, 0.f, 0.f}, sb = {0.f, 0.f, 0.f, 0.f};
      {
        v4u qf[4];
#pragma unroll
        for (int s = 0; s < 4; ++s) qf[s] = *(const LAS v4u*)(L + L_QS + s * 256 + hq * 64 + gq * 16);
        __builtin_amdgcn_sched_barrier(0);
#pragma unroll
        for (int s = 0; s < 4; ++s) { sa = MFMA32(RA[s], qf[s], sa); sb = MFMA32(RA[4 + s], qf[s], sb); }
      }
      constexpr float C2 = 0.08838834764831845f * 1.4426950408889634f;
      float ref = 0.f;
      for (int pass = 0;; ++pass) {
        float p[8];
#pragma unroll
        for (int r = 0; r < 4; ++r) { p[r] = __builtin_amdgcn_exp2f(fmaf(sa[r], C2, -ref)); p[4 + r] = __builtin_amdgcn_exp2f(fmaf(sb[r], C2, -ref)); }
        const v4u pf = {pack_bf16(p[0], p[1]), pack_bf16(p[2], p[3]), pack_bf16(p[4], p[5]), pack_bf16(p[6], p[7])};
        v4u pm[4];
#pragma unroll
        for (int c = 0; c < 4; ++c) { const bool on = (kr >> 2) == c; pm[c] = (v4u){on ? pf.x : 0u, on ? pf.y : 0u, on ? pf.z : 0u, on ? pf.w : 0u}; }
        if (layer == 0 && pass == 0) {
          asm volatile("" : "+v"(pm[0]), "+v"(pm[1]), "+v"(pm[2]), "+v"(pm[3]));
        if (wave >= 4) asm volatile("s_waitcnt vmcnt(0)" ::: "memory");
        lds_barrier();
        if (tid == 0) __builtin_amdgcn_raw_buffer_store_b128((v4u){(u32)__builtin_amdgcn_s_memrealtime(), tag, 0u, tag}, rs, X_HINT + (u32)(g * 16 + jj) * 128, 0, 16);
        const int row0 = wave * 32 + 4 * gq + ((lane & 15) >> 2), vp = lane & 3;
        const u32 va = lbase + L_V + row0 * 256 + ((row0 & 7) << 5) + (vp >> 1) * 16 + (vp & 1) * 8;
        v2u t[16];
        asm volatile(
            "ds_read_b64_tr_b16 %0, %16\n\tds_read_b64_tr_b16 %1, %16 offset:4096\n\tds_read_b64_tr_b16 %2, %17\n\tds_read_b64_tr_b16 %3, %17 offset:4096\n\t"
            "ds_read_b64_tr_b16 %4, %18\n\tds_read_b64_tr_b16 %5, %18 offset:4096\n\tds_read_b64_tr_b16 %6, %19\n\tds_read_b64_tr_b16 %7, %19 offset:4096\n\t"
            "ds_read_b64_tr_b16 %8, %20\n\tds_read_b64_tr_b16 %9, %20 offset:4096\n\tds_read_b64_tr_b16 %10, %21\n\tds_read_b64_tr_b16 %11, %21 offset:4096\n\t"
            "ds_read_b64_tr_b16 %12, %22\n\tds_read_b64_tr_b16 %13, %22 offset:4096\n\tds_read_b64_tr_b16 %14, %23\n\tds_read_b64_tr_b16 %15, %23 offset:4096\n\t"
            "s_waitcnt lgkmcnt(0)"
            : "=&v"(t[0]), "=&v"(t[1]), "=&v"(t[2]), "=&v"(t[3]), "=&v"(t[4]), "=&v"(t[5]), "=&v"(t[6]), "=&v"(t[7]), "=&v"(t[8]), "=&v"(t[9]), "=&v"(t[10]),
              "=&v"(t[11]), "=&v"(t[12]), "=&v"(t[13]), "=&v"(t[14]), "=&v"(t[15])
            : "v"(va), "v"(va ^ 32u), "v"(va ^ 64u), "v"(va ^ 96u), "v"(va ^ 128u), "v"(va ^ 160u), "v"(va ^ 192u), "v"(va ^ 224u) : "memory");
#pragma unroll
        for (int u = 0; u < 8; ++u) RB[u] = (v4u){t[2 * u].x, t[2 * u].y, t[2 * u + 1].x, t[2 * u + 1].y};
      }
        v4f oa = {0.f, 0.f, 0.f, 0.f}, ob = {0.f, 0.f, 0.f, 0.f};
#pragma unroll
        for (int c = 0; c < 4; ++c) { oa = MFMA32(pm[c], RB[c], oa); ob = MFMA32(pm[c], RB[4 + c], ob); }
        const v4f lw = MFMA32(pf, ((v4u){ONES, ONES, ONES, ONES}), ((v4f){0.f, 0.f, 0.f, 0.f}));
        const float ml = fmaxf(fmaxf(fmaxf(fmaxf(sa[0], sa[1]), sa[2]), fmaxf(fmaxf(sa[3], sb[0]), sb[1])), fmaxf(sb[2], sb[3]));
        const bool safe = __all(fabsf(ml) <= SAFE_RANGE / C2);
        *(LAS v4f*)(L + L_OW + wave * 2048 + lane * 16) = oa;
        *(LAS v4f*)(L + L_OW + wave * 2048 + 1024 + lane * 16) = ob;
        if (lane == 0) { *(LAS v4f*)(L + L_ML + wave * 16) = lw; *(LAS u32*)(L + L_ML + 128 + wave * 4) = safe ? 0u : 1u; }
        lds_barrier();
        const v4f x0 = *(const LAS v4f*)(L + L_OW + gq * 2048 + wave * 256 + kr * 16), x1 = *(const LAS v4f*)(L + L_OW + 8192 + gq * 2048 + wave * 256 + kr * 16);
        const u32 fl = *(const LAS u32*)(L + L_ML + (lane & 31) * 4 + (lane & 32) * 4);
        if (isA && pass == 0 && __any(lane >= 32 && lane < 40 && fl != 0u)) {
          float m = tmax32(tmax16(ml * C2));
          if (lane < 4) *(LAS float*)(L + L_P + hq * 32 + wave * 4) = m;
          lds_barrier();
          const v4f m0 = *(const LAS v4f*)(L + L_P + hq * 32), m1 = *(const LAS v4f*)(L + L_P + hq * 32 + 16);
          ref = fmaxf(fmaxf(fmaxf(m0[0], m0[1]), fmaxf(m0[2], m0[3])), fmaxf(fmaxf(m1[0], m1[1]), fmaxf(m1[2], m1[3])));
          continue;
        }
        const v4f xs = x0 + x1;
        const float o = tadd16(tadd32(xs[0], xs[2]), tadd32(xs[1], xs[3]));
        const float o1 = dppf<XOR1>(o);
        const u32 pr = pack_bf16(o, o1);
        const u32 pr2 = (u32)__builtin_amdgcn_mov_dpp((int)pr, XOR2, 0xf, 0xf, true);
        const u32 pbase = xl + X_E2 + (u32)((g * 16 + jj) * 17) * 128;
        if (isA && (lane & 3) == 0) {
          const v4u d = {pr, tag, pr2, tag};
          __builtin_amdgcn_raw_buffer_store_b128(d, rs, pbase + (gq * 4 + (wave >> 1)) * 128 + ((wave & 1) * 4 + (kr >> 2)) * 16, 0, 16);
        }
        if (wave == 7) {
          float lt = __uint_as_float(fl);
          lt += dppf<ROR4>(lt); lt += dppf<ROR8>(lt); lt = tadd16(lt, lt);
          if (isA && lane < 4) {
            const v4u d = {__float_as_uint(ref), tag, __float_as_uint(lt), tag};
            __builtin_amdgcn_raw_buffer_store_b128(d, rs, pbase + 16 * 128 + lane * 16, 0, 16);
          }
        }
        break;
      }
      if (layer == 0 && wave >= 1 && wave < 4) {
        const v4u z = {0u, ~tag, 0u, ~tag};
        int pc = tid - 64; asm volatile("" : "+v"(pc));
        if (pc < 136) __builtin_amdgcn_raw_buffer_store_b128(z, rs, X_LAYER + X_E2 + (u32)((g * 16 + jj) * 17) * 128 + pc * 16, 0, 16);
        else if (isR && pc < 160) { const int line = (pc - 136) >> 3;
          __builtin_amdgcn_raw_buffer_store_b128(z, rs, (line < 2 ? line * X_LAYER + X_E4 : X_E6) + (u32)ri * 128 + (pc & 7) * 16, 0, 16); }
        asm volatile("s_waitcnt vmcnt(0)" ::: "memory");
      }
    }

    if (layer == 0 && (isC | isH)) {
      int ln = tid & 63; asm volatile("" : "+v"(ln));
          asm volatile("s_waitcnt vmcnt(0)" ::: "memory");
          lds_barrier();
          {
            v2u t[16];
            const u32 a0 = tr_addr(0, 0), a1 = tr_addr(0, 1), a2 = tr_addr(1, 0), a3 = tr_addr(1, 1), a4 = tr_addr(2, 0), a5 = tr_addr(2, 1), a6 = tr_addr(3, 0), a7 = tr_addr(3, 1);
            asm volatile(
                "ds_read_b64_tr_b16 %0, %16\n\tds_read_b64_tr_b16 %1, %17\n\tds_read_b64_tr_b16 %2, %16 offset:32768\n\tds_read_b64_tr_b16 %3, %17 offset:32768\n\t"
                "ds_read_b64_tr_b16 %4, %18\n\tds_read_b64_tr_b16 %5, %19\n\tds_read_b64_tr_b16 %6, %18 offset:32768\n\tds_read_b64_tr_b16 %7, %19 offset:32768\n\t"
                "ds_read_b64_tr_b16 %8, %20\n\tds_read_b64_tr_b16 %9, %21\n\tds_read_b64_tr_b16 %10, %20 offset:32768\n\tds_read_b64_tr_b16 %11, %21 offset:32768\n\t"
                "ds_read_b64_tr_b16 %12, %22\n\tds_read_b64_tr_b16 %13, %23\n\tds_read_b64_tr_b16 %14, %22 offset:32768\n\tds_read_b64_tr_b16 %15, %23 offset:32768\n\t"
                "s_waitcnt lgkmcnt(0)"
                : "=&v"(t[0]), "=&v"(t[1]), "=&v"(t[2]), "=&v"(t[3]), "=&v"(t[4]), "=&v"(t[5]), "=&v"(t[6]), "=&v"(t[7]), "=&v"(t[8]), "=&v"(t[9]), "=&v"(t[10]),
                  "=&v"(t[11]), "=&v"(t[12]), "=&v"(t[13]), "=&v"(t[14]), "=&v"(t[15])
                : "v"(a0), "v"(a1), "v"(a2), "v"(a3), "v"(a4), "v"(a5), "v"(a6), "v"(a7) : "memory");
#pragma unroll
            for (int c = 0; c < 4; ++c) {
              RB[2 * c] = (v4u){t[4 * c].x, t[4 * c].y, t[4 * c + 1].x, t[4 * c + 1].y};
              RB[2 * c + 1] = (v4u){t[4 * c + 2].x, t[4 * c + 2].y, t[4 * c + 3].x, t[4 * c + 3].y};
            }
          }
          if (isH) {
            h_cs = prep4(L_W1, RA[0], RA[1], RA[2], RA[3]);
            h_bias = bf_lo(*(const LAS u16*)(L + L_B + (wave * 4 + (ln >> 4)) * 2));
          }
    }
    const bool late = (layer == 0) & isH;
    if (!late) {
      if (wave == 0) {
        if (layer == 0) {
          if (!isA && !isC && !isH) __builtin_amdgcn_s_sleep(D_G0);
          const u32 ho[2] = {X_HINT + (u32)lane * 128, X_HINT + (u32)lane * 128 + 8192};
          v4u hv[2];
          gather<2>(rs, ho, tag, hv, dead, true);
        } else {
          __builtin_amdgcn_s_sleep(D_E2);
        }
        u32 off[8];
#pragma unroll
        for (int i = 0; i < 4; ++i) {
          const u32 pb = xl + X_E2 + (u32)((g * 16 + 4 * i + gq) * 17) * 128;
          off[i] = pb + ci * 256 + kr * 16;
          off[4 + i] = pb + 16 * 128 + (ci >> 1) * 16;
        }
        v4u v[8];
        gather<8>(rs, off, tag, v, dead, layer == 0);
        float M = vmax(vmax3(__uint_as_float(v[4].x), __uint_as_float(v[5].x), __uint_as_float(v[6].x)), __uint_as_float(v[7].x));
        M = tmax16(M); M = tmax32(M);
        float a0 = 0.f, a1 = 0.f, a2 = 0.f, a3 = 0.f, Ls = 0.f;
#pragma unroll
        for (int i = 0; i < 4; ++i) {
          const float e = __builtin_amdgcn_exp2f(__uint_as_float(v[4 + i].x) - M);
          Ls = fmaf(e, __uint_as_float(v[4 + i].z), Ls);
          a0 = fmaf(e, bf_lo(v[i].x), a0); a1 = fmaf(e, bf_hi(v[i].x), a1); a2 = fmaf(e, bf_lo(v[i].z), a2); a3 = fmaf(e, bf_hi(v[i].z), a3);
        }
        float b0 = tadd32(a0, a2), b1 = tadd32(a1, a3); Ls = tadd32(Ls, Ls);
        b0 = tadd16(b0, b0); b1 = tadd16(b1, b1); Ls = tadd16(Ls, Ls);
        const float inv = __builtin_amdgcn_rcpf(Ls);
        if (!(lane & 16)) *(LAS u32*)(L + L_OS + kr * 8 + (lane >> 5) * 4) = pack_bf16(b0 * inv, b1 * inv);
      }
      lds_barrier();
      const v4u of0 = *(const LAS v4u*)(L + L_OS + gq * 16), of1 = *(const LAS v4u*)(L + L_OS + 64 + gq * 16);
      v4f acc[4];
#pragma unroll
      for (int c = 0; c < 4; ++c) acc[c] = MFMA32(of1, RB[2 * c + 1], MFMA32(of0, RB[2 * c], ((v4f){0.f, 0.f, 0.f, 0.f})));
      store_partial(acc[0][0], acc[1][0], acc[2][0], acc[3][0], xl + X_E3 + (u32)(g * 8 + ci) * 4096, isC);
    }

#pragma unroll
    for (int ph = 0; ph < 2; ++ph) {
      lds_barrier();
      if (wave == 0 && !(late && ph == 0)) {
        constexpr int NP = 8;
        if (ph == 0) __builtin_amdgcn_s_sleep(D_E3); else __builtin_amdgcn_s_sleep(D_E5);
        u32 off[NP];
#pragma unroll
        for (int i = 0; i < NP; ++i) off[i] = xl + (ph == 0 ? X_E3 : X_E5) + (u32)(((ph == 1 && i == 7 ? 6 : i) * 8 + (lane >> 3)) * 32 + ri) * 128 + (lane & 7) * 16;
        const u32 xin = ph == 0 ? *(const LAS u32*)(L + L_XS + ri * 32 + (lane & 7) * 4) : xkeep;
        const u32 bb = *(const LAS u32*)(L + L_B + 64 + (lane & 7) * 4);
        float s0, s1;
        int n = 0;
        for (;; ++n) {
          asm volatile("" ::: "memory");
          v4u v[NP];
#pragma unroll
          for (int i = 0; i < (ph == 0 ? 8 : 7); ++i) v[i] = __builtin_amdgcn_raw_buffer_load_b128(rs, off[i], 0, 16);
          u32 bad = 0u; s0 = 0.f; s1 = 0.f;
#pragma unroll
          for (int i = 0; i < (ph == 0 ? 8 : 7); ++i) {
            bad |= (v[i].y ^ tag); bad |= (v[i].w ^ tag); s0 += __uint_as_float(v[i].x); s1 += __uint_as_float(v[i].z);
            asm volatile("" : "+v"(bad), "+v"(s0), "+v"(s1));
          }
          if (__all(bad == 0u)) break;
          if (dead || n > SPIN_CAP) { dead = true; break; }
        }
        float t = tadd32(s0, s1); t = tadd16(t, t); t += dppf<ROR8>(t);
        float r = rbf(t);
        if (ph == 1) r = rbf(r + ((lane & 32) ? bf_hi(bb) : bf_lo(bb)));
        r = r + ((lane & 32) ? bf_hi(xin) : bf_lo(xin));
        auto sw = __builtin_amdgcn_permlane32_swap(__float_as_uint(r), __float_as_uint(r), false, false);
        const u32 pr = pack_bf16(__uint_as_float(sw[0]), __uint_as_float(sw[1]));
        xkeep = pr;
        if (isR && lane < 8) {
          if (ph == 0 || layer == 0) {
            const v2u d = {pr, tag};
            __builtin_amdgcn_raw_buffer_store_b64(d, rs, xl + (ph == 0 ? X_E4 : X_E6) + (u32)ri * 128 + lane * 8, 0, 16);
          } else {
            *(u32*)(a.out + ri * 16 + lane * 2) = pr;
          }
        }
      }
      if (ph == 0) {
        if (wave == 0) {
          if (late) {
            const u32 ho[2] = {X_HINT + (u32)lane * 128, X_HINT + (u32)lane * 128 + 8192};
            v4u hv[2];
            gather<2>(rs, ho, tag, hv, dead, true);
            const u32 now = (u32)__builtin_amdgcn_s_memrealtime();
            float age = fminf((float)(int)(now - hv[0].x), (float)(int)(now - hv[1].x));
            age = fminf(age, dppf<ROR8>(age)); age = fminf(age, dppf<ROR4>(age)); age = fminf(age, dppf<ROR2>(age)); age = fminf(age, dppf<ROR1>(age));
            age = -tmax32(tmax16(-age));
            const int wait = T_H4 - (int)rdlane(age, 0);
            for (int i = 0; i < 4096 && (int)((u32)__builtin_amdgcn_s_memrealtime() - now) < wait; ++i) {}
          } else {
            __builtin_amdgcn_s_sleep(D_E4);
          }
          const u32 o0 = xl + X_E4 + (lane >> 1) * 128 + (lane & 1) * 32;
          const u32 off[2] = {o0, o0 + 16};
          v4u v[2];
          gather<2>(rs, off, tag, v, dead);
          *(LAS v4u*)(L + L_XP + lane * 16) = (v4u){v[0].x, v[0].z, v[1].x, v[1].z};
        }
        lds_barrier();
        const v4u xv = *(const LAS v4u*)(L + L_XP + lane * 16);
        const v4u w1r[4] = {RA[0], RA[1], RA[2], RA[3]};
        const float z = rbf(rbf(ln_gemv4(xv, w1r, h_cs)) + h_bias);
        const float u2 = z * (1.5957691216057308f + 0.07135481627260025f * z * z);
        const float y = z * __builtin_amdgcn_rcpf(1.f + __builtin_amdgcn_exp2f(-1.4426950408889634f * u2));
        if (kr == 0) *(LAS u16*)(L + L_HS + (wave * 4 + gq) * 2) = (u16)pack_bf16(y, y);
        lds_barrier();
        const v4u hf = *(const LAS v4u*)(L + L_HS + gq * 16);
        v4f acc[4];
#pragma unroll
        for (int c = 0; c < 4; ++c) acc[c] = MFMA32(hf, RB[2 * c], ((v4f){0.f, 0.f, 0.f, 0.f}));
        store_partial(acc[0][0], acc[1][0], acc[2][0], acc[3][0], xl + X_E5 + (u32)hidx * 4096, isH);
      }
    }
  }
  if (b == 0 && tid == 0) __hip_atomic_store((u32*)a.ws, tag, __ATOMIC_RELAXED, __HIP_MEMORY_SCOPE_AGENT);
}

}

extern "C" void kernel_launch(void* const* d_in, const int* in_sizes, int n_in, void* d_out, int out_size, void* d_ws,
                              size_t ws_size, hipStream_t stream) {
  Args a;
  a.x = (const u16*)d_in[0]; a.kc = (const u16*)d_in[1]; a.vc = (const u16*)d_in[2]; a.wq = (const u16*)d_in[3];
  a.wo = (const u16*)d_in[4]; a.w1 = (const u16*)d_in[6]; a.b1 = (const u16*)d_in[7]; a.w2 = (const u16*)d_in[8];
  a.b2 = (const u16*)d_in[9]; a.out = (u16*)d_out; a.ws = (unsigned char*)d_ws;
  fused_decode<<<NWG, NTHR, 0, stream>>>(a);
}
```
